```python
import torch

def _softmax(x, axis=-1):
    return torch.softmax(x, dim=axis)

def _gelu(x):
    return torch.nn.functional.gelu(x, approximate='tanh')

def _silu(x):
    return torch.nn.functional.silu(x)

def _sigmoid(x):
    return torch.sigmoid(x)

def _layernorm(x, axis=-1, eps=1e-05):
    m = x.mean(dim=axis, keepdim=True)
    v = x.var(dim=axis, keepdim=True, unbiased=False)
    return (x - m) / torch.sqrt(v + eps)

def _scatter_add(tgt, idx, src, axis=0):
    return tgt.clone().index_add_(axis, idx, src)

def _segment_sum(src, seg, n):
    out = torch.zeros((n,) + src.shape[1:], dtype=src.dtype)
    return out.index_add_(0, seg, src)

def _pad(x, pw):
    flat = tuple((v for pair in reversed(pw) for v in pair))
    return torch.nn.functional.pad(x, flat)
kernel_name = 'adaLN_cond__mha_block__dense_activation_silu__prompt'

def setup_inputs():

    _w = lambda a: (lambda t: t.to(torch.bfloat16) if t.is_floating_point() else t)(torch.as_tensor(a))

    def _A_setup():
        B, S, D, H = (1, 2048, 576, 32)
        return {'x': _w((lambda *s: torch.randn(*s, dtype=torch.bfloat16) * 0.05)(B, S, D)), 'wqkv': _w((lambda *s: torch.randn(*s, dtype=torch.bfloat16) * 0.05)(D, 3 * D)), 'wo': _w((lambda *s: torch.randn(*s, dtype=torch.bfloat16) * 0.05)(D, D))}
    _A_in = _A_setup()

    def _B_setup():
        B, S, D, F = (1, 2048, 576, 4608)
        return {'x': _w((lambda *s: torch.randn(*s, dtype=torch.bfloat16) * 0.05)(B, S, D)), 'w1': _w((lambda *s: torch.randn(*s, dtype=torch.bfloat16) * 3.2)(D, F) * D ** (-0.5)), 'w2': _w((lambda *s: torch.randn(*s, dtype=torch.bfloat16) * 0.4)(F, D) * F ** (-0.5))}
    _B_in = _B_setup()
    return {'A_x': _A_in['x'], 'A_wqkv': _A_in['wqkv'], 'A_wo': _A_in['wo'], 'B_x': _B_in['x'], 'B_w1': _B_in['w1'], 'B_w2': _B_in['w2'], 'c': _w((lambda *s: torch.randn(*s, dtype=torch.bfloat16) * 0.1)(1, 576) * 8.0), 'w_mod': _w((lambda *s: torch.randn(*s, dtype=torch.bfloat16) * 0.1)(576, 6 * 576) * 576 ** (-0.5) * 8.0)}

def reference(A_x, A_wqkv, A_wo, B_x, B_w1, B_w2, c, w_mod):

    def _A_ref(x, wqkv, wo):
        B, S, D = x.shape
        H = 32
        Dh = D // H
        qkv = (lambda x, *d: x.permute(*d))((x @ wqkv).reshape(B, S, 3, H, Dh), 2, 0, 3, 1, 4)
        q, k, v = (qkv[0], qkv[1], qkv[2])
        scores = torch.einsum('bhqd,bhkd->bhqk', q, k) * Dh ** (-0.5)
        p = _softmax(scores, axis=-1)
        o = torch.einsum('bhqk,bhkd->bhqd', p, v)
        return (lambda x, *d: x.permute(*d))(o, 0, 2, 1, 3).reshape(B, S, D) @ wo

    def _B_ref(x, w1, w2):
        h = torch.einsum('bsd,df->bsf', x, w1)
        h = _silu(h)
        out = torch.einsum('bsf,fd->bsd', h, w2)
        return out
    x = A_x
    mod = (_silu(c) @ w_mod).reshape(c.shape[0], 6, 1, -1)
    sa, ca, ga, sb, cb, gb = (mod[:, 0].clone(), mod[:, 1].clone(), mod[:, 2].clone(), mod[:, 3].clone(), mod[:, 4].clone(), mod[:, 5].clone())
    h = _layernorm(x, axis=-1) * (1 + ca) + sa
    x = x + ga * _A_ref(h, A_wqkv, A_wo)
    h = _layernorm(x, axis=-1) * (1 + cb) + sb
    x = x + gb * _B_ref(h, B_w1, B_w2)
    return x
```

```cpp
#include <hip/hip_runtime.h>
#include <hip/hip_bf16.h>
typedef __hip_bfloat16 bf16;
constexpr int S=2048, D=576, H=32, DH=18, F=4608;
__device__ inline float b2f(bf16 x){return __bfloat162float(x);}
__device__ inline bf16 f2b(float x){return __float2bfloat16(x);}
__device__ inline float rb(float x){return __bfloat162float(__float2bfloat16(x));}

__global__ void k_mod(const bf16* c,const bf16* wmod,float* mod){
  int j=blockIdx.x*blockDim.x+threadIdx.x; if(j>=6*D) return;
  float s=0; for(int k=0;k<D;k++){float cv=b2f(c[k]); float sv=rb(cv/(1.f+__expf(-cv))); s+=sv*b2f(wmod[k*6*D+j]);}
  mod[j]=rb(s);
}
__global__ void k_ln(const bf16* x,const float* shift,const float* scale,bf16* h){
  int r=blockIdx.x; __shared__ float red[64];
  float v[9]; float s=0; int t=threadIdx.x;
  for(int i=0;i<9;i++){v[i]=b2f(x[r*D+t+64*i]); s+=v[i];}
  for(int o=32;o>0;o>>=1) s+=__shfl_xor(s,o);
  float m=s/D; float q=0; for(int i=0;i<9;i++){float d=v[i]-m;q+=d*d;}
  for(int o=32;o>0;o>>=1) q+=__shfl_xor(q,o);
  float rs=rsqrtf(q/D+1e-5f);
  for(int i=0;i<9;i++){int k=t+64*i; h[r*D+k]=f2b((v[i]-m)*rs*(1.f+scale[k])+shift[k]);}
}
template<int MODE>
__global__ void k_gemm(const bf16* A,const bf16* B,bf16* C,int M,int N,int K,const bf16* res,const float* gate){
  __shared__ float As[16][17],Bs[16][17];
  int tx=threadIdx.x&15,ty=threadIdx.x>>4; int row=blockIdx.y*16+ty,col=blockIdx.x*16+tx; float s=0;
  for(int k0=0;k0<K;k0+=16){As[ty][tx]=b2f(A[row*K+k0+tx]);Bs[ty][tx]=b2f(B[(k0+ty)*N+col]);__syncthreads();
    for(int k=0;k<16;k++) s+=As[ty][k]*Bs[k][tx]; __syncthreads();}
  if(MODE==0) C[row*N+col]=f2b(s);
  if(MODE==1){float h=rb(s); C[row*N+col]=f2b(h/(1.f+__expf(-h)));}
  if(MODE==2){C[row*N+col]=f2b(b2f(res[row*N+col])+rb(gate[col]*rb(s)));}
}
__global__ void k_attn(const bf16* qkv,bf16* o){
  int h=blockIdx.y; int q=blockIdx.x*64+threadIdx.x;
  float qv[DH]; for(int d=0;d<DH;d++) qv[d]=b2f(qkv[q*3*D+h*DH+d]);
  float m=-1e30f,l=0,acc[DH]; for(int d=0;d<DH;d++) acc[d]=0;
  __shared__ float ks[64][DH],vs[64][DH];
  for(int k0=0;k0<S;k0+=64){
    for(int d=0;d<DH;d++){ks[threadIdx.x][d]=b2f(qkv[(k0+threadIdx.x)*3*D+D+h*DH+d]);vs[threadIdx.x][d]=b2f(qkv[(k0+threadIdx.x)*3*D+2*D+h*DH+d]);}
    __syncthreads();
    for(int k=0;k<64;k++){float s=0;for(int d=0;d<DH;d++) s+=qv[d]*ks[k][d]; s*=0.23570226039551584f;
      float mn=fmaxf(m,s); float a=__expf(m-mn),p=__expf(s-mn); l=l*a+p; for(int d=0;d<DH;d++) acc[d]=acc[d]*a+p*vs[k][d]; m=mn;}
    __syncthreads();
  }
  for(int d=0;d<DH;d++) o[q*D+h*DH+d]=f2b(acc[d]/l);
}
extern "C" void kernel_launch(void* const* d_in,const int* in_sizes,int n_in,void* d_out,int out_size,void* d_ws,size_t ws_size,hipStream_t stream){
  const bf16* x=(const bf16*)d_in[0]; const bf16* wqkv=(const bf16*)d_in[1]; const bf16* wo=(const bf16*)d_in[2];
  const bf16* w1=(const bf16*)d_in[4]; const bf16* w2=(const bf16*)d_in[5]; const bf16* c=(const bf16*)d_in[6]; const bf16* wmod=(const bf16*)d_in[7];
  char* ws=(char*)d_ws; float* mod=(float*)ws; bf16* h=(bf16*)(ws+(1<<20)); bf16* qkv=(bf16*)(ws+(8<<20)); bf16* o=(bf16*)(ws+(24<<20)); bf16* x1=(bf16*)(ws+(32<<20)); bf16* h1=(bf16*)(ws+(40<<20));
  bf16* out=(bf16*)d_out;
  k_mod<<<(6*D+255)/256,256,0,stream>>>(c,wmod,mod);
  k_ln<<<S,64,0,stream>>>(x,mod,mod+D,h);
  k_gemm<0><<<dim3(3*D/16,S/16),256,0,stream>>>(h,wqkv,qkv,S,3*D,D,nullptr,nullptr);
  k_attn<<<dim3(S/64,H),64,0,stream>>>(qkv,o);
  k_gemm<2><<<dim3(D/16,S/16),256,0,stream>>>(o,wo,x1,S,D,D,x,mod+2*D);
  k_ln<<<S,64,0,stream>>>(x1,mod+3*D,mod+4*D,h);
  k_gemm<1><<<dim3(F/16,S/16),256,0,stream>>>(h,w1,h1,S,F,D,nullptr,nullptr);
  k_gemm<2><<<dim3(D/16,S/16),256,0,stream>>>(h1,w2,out,S,D,F,x1,mod+5*D);
}
```

```cpp
#include <hip/hip_runtime.h>
#include <hip/hip_bf16.h>
#include <stdint.h>

typedef unsigned short u16;
typedef short bf16x8 __attribute__((ext_vector_type(8)));
typedef short s16x4 __attribute__((ext_vector_type(4)));
typedef float f32x4 __attribute__((ext_vector_type(4)));
typedef float f32x16 __attribute__((ext_vector_type(16)));
typedef unsigned u32x4 __attribute__((ext_vector_type(4)));
typedef unsigned u32x2 __attribute__((ext_vector_type(2)));
#define LAS __attribute__((address_space(3)))
#define GAS __attribute__((address_space(1)))
#define DI __device__ __forceinline__

constexpr int S = 2048, D = 576, NH = 32, DH = 18, F = 4608;
constexpr int KS = 18;

constexpr size_t MODP_OFF = 0;
constexpr size_t WQKVT_OFF = 0x10000;
constexpr size_t WOT_OFF = WQKVT_OFF + 8 * 14 * 18 * 1024;
constexpr size_t W1T_OFF = WOT_OFF + 36 * 18 * 1024;
constexpr size_t W2T_OFF = W1T_OFF + 8 * 648 * 1024;
constexpr size_t QLO_OFF = W2T_OFF + 8 * 648 * 1024;
constexpr size_t QHI_OFF = QLO_OFF + 32 * 64 * 1024;
constexpr int KVT = 2432;
constexpr int KVH = 64 * KVT;
constexpr size_t KV_OFF = QHI_OFF + 32 * 2048 * 4;
constexpr size_t OLO_OFF = KV_OFF + (size_t)32 * KVH;
constexpr size_t OHI_OFF = OLO_OFF + 32 * 2048 * 32;
constexpr size_t X1_OFF = OHI_OFF + 8 * 2048 * 16;
constexpr size_t PART_OFF = X1_OFF + (size_t)S * D * 2;
constexpr size_t WS_END = PART_OFF + (size_t)8 * S * D * 2;

DI float bf2f(unsigned v) { return __builtin_bit_cast(float, v << 16); }
DI float bflo(unsigned v) { return __builtin_bit_cast(float, v << 16); }
DI float bfhi(unsigned v) { return __builtin_bit_cast(float, v & 0xffff0000u); }
typedef __bf16 bf16x2_t __attribute__((ext_vector_type(2)));
typedef float f32x2 __attribute__((ext_vector_type(2)));
DI unsigned cvtpk(float lo, float hi) {
  return __builtin_bit_cast(unsigned, __builtin_convertvector(f32x2{lo, hi}, bf16x2_t));
}
DI float rbf(float x) { return bflo(cvtpk(x, x)); }
DI f32x4 mfma16(u32x4 a, u32x4 b, f32x4 c) {
  return __builtin_amdgcn_mfma_f32_16x16x32_bf16(__builtin_bit_cast(bf16x8, a), __builtin_bit_cast(bf16x8, b), c, 0, 0, 0);
}
DI f32x16 mfma32(u32x4 a, u32x4 b, f32x16 c) {
  return __builtin_amdgcn_mfma_f32_32x32x16_bf16(__builtin_bit_cast(bf16x8, a), __builtin_bit_cast(bf16x8, b), c, 0, 0, 0);
}
DI void glds16(const void* g, unsigned lds_base) {
  unsigned sv;
  asm volatile("s_mov_b32 %0, m0\n\ts_mov_b32 m0, %2\n\ts_nop 0\n\tglobal_load_lds_dwordx4 %1, off\n\ts_mov_b32 m0, %0"
               : "=&s"(sv) : "v"(g), "s"(lds_base) : "memory");
}
#define WAIT_VM(n) asm volatile("s_waitcnt vmcnt(" #n ")" ::: "memory")
#define SBAR() __builtin_amdgcn_sched_barrier(0)
DI float silu_f(float x) { return x * __builtin_amdgcn_rcpf(1.f + __builtin_amdgcn_exp2f(-1.4426950408889634f * x)); }
typedef short v4i16_t __attribute__((ext_vector_type(4)));
DI u32x2 tr_read(const LAS char* p) {
  return __builtin_bit_cast(u32x2, __builtin_amdgcn_ds_read_tr16_b64_v4i16((LAS v4i16_t*)p));
}
DI u32x4 lds128(const LAS char* p) { return *(const LAS u32x4*)p; }

DI int frag_index(int T, int s) {
  return T < 20 ? (T / 5) * 90 + s * 5 + (T % 5) : 360 + ((T - 20) >> 2) * 72 + s * 4 + ((T - 20) & 3);
}

constexpr int K0_MOD = 216, K0_U1 = 1296, K0_U2 = 1296, K0_UO = 162, K0_UNITS = K0_U1 + K0_U2 + K0_UO;
constexpr int K0_TILE_BLKS = (K0_UNITS + 3) / 4, K0_QKV_BLKS = 2016 / 4;
__global__ void __launch_bounds__(256) k0_prep(const u16* __restrict__ c, const u16* __restrict__ wmod, const u16* __restrict__ wqkv,
                                               const u16* __restrict__ wo, const u16* __restrict__ w1, const u16* __restrict__ w2,
                                               char* __restrict__ ws) {
  __shared__ __attribute__((aligned(16))) char lds[16384];
  const int tid = threadIdx.x, lane = tid & 63, w = __builtin_amdgcn_readfirstlane(tid >> 6);
  int b = blockIdx.x;
  if (b < K0_MOD) {
    const int cc = b % 54, ks = b / 54;
    const int ch = tid & 7, rg = tid >> 3;
    float acc[8];
#pragma unroll
    for (int j = 0; j < 8; ++j) acc[j] = 0.f;
    u32x4 wv[5];
    float sv[5];
#pragma unroll
    for (int i = 0; i < 5; ++i) {
      int r = rg + 32 * i;
      bool ok = r < 144;
      int k = ks * 144 + (ok ? r : 0);
      wv[i] = *(const u32x4*)(wmod + (size_t)k * 3456 + cc * 64 + ch * 8);
      float cv = bf2f(c[k]);
      sv[i] = ok ? rbf(cv / (1.f + __expf(-cv))) : 0.f;
    }
#pragma unroll
    for (int i = 0; i < 5; ++i)
#pragma unroll
      for (int j = 0; j < 4; ++j) {
        acc[2 * j] += sv[i] * bflo(wv[i][j]);
        acc[2 * j + 1] += sv[i] * bfhi(wv[i][j]);
      }
#pragma unroll
    for (int j = 0; j < 8; ++j) {
      acc[j] += __shfl_xor(acc[j], 8);
      acc[j] += __shfl_xor(acc[j], 16);
      acc[j] += __shfl_xor(acc[j], 32);
    }
    float* red = (float*)lds;
    if (lane < 8)
#pragma unroll
      for (int j = 0; j < 8; ++j) red[w * 64 + lane * 8 + j] = acc[j];
    __syncthreads();
    if (tid < 64) ((float*)(ws + MODP_OFF))[ks * 3456 + cc * 64 + tid] = red[tid] + red[64 + tid] + red[128 + tid] + red[192 + tid];
    return;
  }
  b -= K0_MOD;
  if (b < K0_TILE_BLKS) {
    int u = b * 4 + w;
    if (u >= K0_UNITS) return;
    const u16* src; int ldn, n0, kind, s, fr = 0;
    if (u < K0_U1) { kind = 0; s = u % 18; n0 = (u / 18) * 64; src = w1; ldn = F; }
    else if (u < K0_U1 + K0_U2) { int v = u - K0_U1; kind = 1; int sg = v % 144; fr = sg / 18; s = sg % 18; n0 = (v / 144) * 64; src = w2; ldn = D; }
    else { int v = u - K0_U1 - K0_U2; kind = 2; s = v % 18; n0 = (v / 18) * 64; src = wo; ldn = D; }
    const unsigned lb = (unsigned)(size_t)(LAS char*)lds + w * 4096;
#pragma unroll
    for (int p = 0; p < 4; ++p) {
      int rr = 8 * p + (lane >> 3);
      int g = rr >> 3, j = rr & 7, k;
      if (kind == 0) k = 32 * s + rr;
      else if (kind == 1) k = 576 * fr + 32 * s + 16 * (j >> 2) + 4 * g + (j & 3);
      else { int ci = 4 * s + g, G = ci / 9, r9 = ci % 9; k = r9 < 8 ? (4 * G + (r9 >> 1)) * 18 + 8 * (r9 & 1) + j : (4 * G + (j >> 1)) * 18 + 16 + (j & 1); }
      glds16(src + (size_t)k * ldn + n0 + (lane & 7) * 8, lb + p * 1024);
    }
    WAIT_VM(0);
    const LAS char* img = (const LAS char*)lds + w * 4096;
    const int g = lane >> 4, q4 = (lane & 15) >> 2, p4 = lane & 3;
#pragma unroll
    for (int i = 0; i < 4; ++i) {
      u32x2 lo = tr_read(img + (8 * g + q4) * 128 + i * 32 + p4 * 8);
      u32x2 hi = tr_read(img + (8 * g + 4 + q4) * 128 + i * 32 + p4 * 8);
      u32x4 fragv = {lo[0], lo[1], hi[0], hi[1]};
      int Tn = n0 / 16 + i;
      size_t off;
      if (kind == 0) off = W1T_OFF + ((size_t)(Tn / 36) * 648 + frag_index(Tn % 36, s)) * 1024;
      else if (kind == 1) off = W2T_OFF + ((size_t)fr * 648 + frag_index(Tn, s)) * 1024;
      else off = WOT_OFF + ((size_t)Tn * 18 + s) * 1024;
      *(u32x4*)(ws + off + lane * 16) = fragv;
    }
    return;
  }
  b -= K0_TILE_BLKS;
  {
    int u = b * 4 + w;
    int s = u % 18, tile = (u / 18) % 14, hq = u / (18 * 14);
    int g = lane >> 4, r = lane & 15;
    int col; bool ok = true;
    if (tile < 12) col = (tile >> 2) * 576 + (4 * hq + (tile & 3)) * 18 + r;
    else if (tile == 12) { int i = r >> 1; col = (i >> 2) * 576 + (4 * hq + (i & 3)) * 18 + 16 + (r & 1); }
    else { ok = r < 8; int i = (r >> 1) & 3; col = 1152 + (4 * hq + i) * 18 + 16 + (r & 1); }
    u16 v[8];
#pragma unroll
    for (int j = 0; j < 8; ++j) v[j] = wqkv[(size_t)(32 * s + 8 * g + j) * 1728 + col];
    u32x4 o;
#pragma unroll
    for (int j = 0; j < 4; ++j) o[j] = ok ? ((unsigned)v[2 * j] | ((unsigned)v[2 * j + 1] << 16)) : 0u;
    *(u32x4*)(ws + WQKVT_OFF + (size_t)u * 1024 + lane * 16) = o;
  }
}

DI void ln_load(const u16* __restrict__ x, int row0, int w, int lane, u32x4 (&xv)[9]) {
  const u16* p = x + (size_t)(row0 + 8 * w + (lane >> 3)) * D + (lane & 7) * 8;
#pragma unroll
  for (int i = 0; i < 9; ++i) xv[i] = *(const u32x4*)(p + 64 * i);
}
DI void ln_finish(const u32x4 (&xv)[9], const LAS float* modv, LAS char* hb, int w, int lane) {
  float s = 0.f;
#pragma unroll
  for (int i = 0; i < 9; ++i)
#pragma unroll
    for (int j = 0; j < 4; ++j) s += bflo(xv[i][j]) + bfhi(xv[i][j]);
  s += __shfl_xor(s, 1); s += __shfl_xor(s, 2); s += __shfl_xor(s, 4);
  const float mean = s * (1.f / D);
  float q = 0.f;
#pragma unroll
  for (int i = 0; i < 9; ++i)
#pragma unroll
    for (int j = 0; j < 4; ++j) { float a = bflo(xv[i][j]) - mean, b = bfhi(xv[i][j]) - mean; q += a * a; q += b * b; }
  q += __shfl_xor(q, 1); q += __shfl_xor(q, 2); q += __shfl_xor(q, 4);
  const float rstd = __builtin_amdgcn_rsqf(q * (1.f / D) + 1e-5f);
  const float nb = -mean * rstd;
  const int tk = 8 * w + (lane >> 3), tt = tk >> 4, c = tk & 15;
#pragma unroll
  for (int i = 0; i < 9; ++i) {
    const int ch = (lane & 7) + 8 * i, sidx = ch >> 2, g = ch & 3;
    const LAS f32x4* gp = (const LAS f32x4*)(modv + ch * 8);
    const LAS f32x4* sp = (const LAS f32x4*)(modv + 576 + ch * 8);
    f32x4 g0 = gp[0], g1 = gp[1], s0 = sp[0], s1 = sp[1];
    u32x4 o;
    o[0] = cvtpk((bflo(xv[i][0]) * rstd + nb) * g0[0] + s0[0], (bfhi(xv[i][0]) * rstd + nb) * g0[1] + s0[1]);
    o[1] = cvtpk((bflo(xv[i][1]) * rstd + nb) * g0[2] + s0[2], (bfhi(xv[i][1]) * rstd + nb) * g0[3] + s0[3]);
    o[2] = cvtpk((bflo(xv[i][2]) * rstd + nb) * g1[0] + s1[0], (bfhi(xv[i][2]) * rstd + nb) * g1[1] + s1[1]);
    o[3] = cvtpk((bflo(xv[i][3]) * rstd + nb) * g1[2] + s1[2], (bfhi(xv[i][3]) * rstd + nb) * g1[3] + s1[3]);
    *(LAS u32x4*)(hb + ((sidx * 4 + tt) * 64 + g * 16 + (c ^ (ch & 7))) * 16) = o;
  }
}
DI void mod_to_lds(const float* __restrict__ modp, int which, LAS float* modv, int tid, int nthr) {
  for (int i = tid; i < 2 * D; i += nthr) {
    const int isshift = i >= D, k = isshift ? i - D : i;
    const int col = (which + (isshift ? 0 : 1)) * D + k;
    const float v = rbf(modp[col] + modp[3456 + col] + modp[2 * 3456 + col] + modp[3 * 3456 + col]);
    modv[i] = isshift ? v : rbf(1.f + v);
  }
}
DI float mod_val(const float* __restrict__ modp, int col) {
  return rbf(modp[col] + modp[3456 + col] + modp[2 * 3456 + col] + modp[3 * 3456 + col]);
}
DI const LAS char* bfrag_base(const LAS char* hb, int lane, int par) {
  const int g = lane >> 4, c = lane & 15;
  return hb + (g * 16 + (c ^ ((4 * par + g) & 7))) * 16;
}

constexpr float QSCALE = 0.23570226039551584f * 1.4426950408889634f;
__global__ void __launch_bounds__(512, 2) k1_qkv(const u16* __restrict__ x, char* __restrict__ ws) {
  __shared__ __attribute__((aligned(16))) char lds[73728 + 4608];
  const int tid = threadIdx.x, lane = tid & 63, w = __builtin_amdgcn_readfirstlane(tid >> 6);
  const int hq = blockIdx.x & 7, rb = blockIdx.x >> 3;
  LAS char* hb = (LAS char*)lds;
  LAS float* modv = (LAS float*)(hb + 73728);
  u32x4 xv[9];
  ln_load(x, rb * 64, w, lane, xv);
  mod_to_lds((const float*)(ws + MODP_OFF), 0, modv, tid, 512);
  constexpr int WIN = 16;
  u32x4 a[WIN];
  const int wt = w < 7 ? w : 6;
  const u32x4* ap = (const u32x4*)(ws + WQKVT_OFF + ((size_t)hq * 14 + 2 * wt) * 18 * 1024) + lane;
#pragma unroll
  for (int i = 0; i < WIN; ++i) a[i] = ap[((i & 1) * 18 + (i >> 1)) * 64];
  SBAR();
  __syncthreads();
  ln_finish(xv, modv, hb, w, lane);
  __syncthreads();
  if (w == 7) return;
  f32x4 acc[2][4];
#pragma unroll
  for (int j = 0; j < 2; ++j)
#pragma unroll
    for (int t = 0; t < 4; ++t) acc[j][t] = f32x4{0.f, 0.f, 0.f, 0.f};
  const LAS char* b0 = bfrag_base(hb, lane, 0);
  const LAS char* b1 = bfrag_base(hb, lane, 1);
  const bool n0 = (w == 4 || w == 5), n1 = (w >= 4);
#pragma unroll
  for (int s = 0; s < KS; ++s) {
    u32x4 bf[4];
#pragma unroll
    for (int t = 0; t < 4; ++t) bf[t] = lds128(((s & 1) ? b1 : b0) + (s * 4 + t) * 1024);
#pragma unroll
    for (int j = 0; j < 2; ++j) {
      const int i = 2 * s + j;
      const u32x4 cur = a[i % WIN];
      if (i + WIN < 36) a[i % WIN] = ap[(((i + WIN) & 1) * 18 + ((i + WIN) >> 1)) * 64];
      if (j == 0 ? n0 : n1) {
#pragma unroll
        for (int t = 0; t < 4; ++t) acc[j][t] = mfma16(bf[t], cur, acc[j][t]);
      } else {
#pragma unroll
        for (int t = 0; t < 4; ++t) acc[j][t] = mfma16(cur, bf[t], acc[j][t]);
      }
    }
    SBAR();
  }
  const int g = lane >> 4, c = lane & 15;
#pragma unroll
  for (int j = 0; j < 2; ++j) {
    const int tile = 2 * w + j;
#pragma unroll
    for (int t = 0; t < 4; ++t) {
      const f32x4 v = acc[j][t];
      if (tile < 8) {
        const int tok = rb * 64 + t * 16 + c, h = 4 * hq + (tile & 3);
        const float sc = tile < 4 ? QSCALE : 1.f;
        u32x2 o = {cvtpk(v[0] * sc, v[1] * sc), cvtpk(v[2] * sc, v[3] * sc)};
        const int sub = ((g >> 1) * 32 + (tok & 31)) * 16 + (g & 1) * 8;
        char* dst = tile < 4 ? ws + QLO_OFF + ((size_t)h * 64 + (tok >> 5)) * 1024 + sub
                             : ws + KV_OFF + (size_t)h * KVH + (tok >> 5) * KVT + sub;
        *(u32x2*)dst = o;
      } else if (tile < 12 || tile == 13) {
        const int tok0 = rb * 64 + t * 16, kt = tok0 >> 5, sstep = (tok0 >> 4) & 1;
        int h, d; u32x2 o = {cvtpk(v[0], v[1]), cvtpk(v[2], v[3])};
        if (tile < 12) { h = 4 * hq + (tile & 3); d = c; }
        else if (c < 8) { h = 4 * hq + (c >> 1); d = 16 + (c & 1); }
        else { h = 4 * hq + (c & 3); d = c < 12 ? 18 : 19; const unsigned cv = c < 12 ? 0x3F803F80u : 0u; o = u32x2{cv, cv}; }
        *(u32x2*)(ws + KV_OFF + (size_t)h * KVH + kt * KVT + 1152 + sstep * 640 + (g & 1) * 320 + d * 16 + (g >> 1) * 8) = o;
      } else {
        const int tok = rb * 64 + t * 16 + c;
        const bool isq = g < 2;
        const float sc = isq ? QSCALE : 1.f;
#pragma unroll
        for (int e = 0; e < 2; ++e) {
          const int h = 4 * hq + 2 * (g & 1) + e;
          const unsigned o = cvtpk(v[2 * e] * sc, v[2 * e + 1] * sc);
          char* dst = isq ? ws + QHI_OFF + ((size_t)h * 2048 + tok) * 4
                          : ws + KV_OFF + (size_t)h * KVH + (tok >> 5) * KVT + 1024 + (tok & 31) * 4;
          *(unsigned*)dst = o;
        }
      }
    }
  }
}

template <bool SUBMAX>
DI void attn_tiles(const LAS char* kv, int t0, int t1, u32x4 q0, u32x4 q1, float negm, int lane, f32x16& o) {
  const LAS char* kp = kv + lane * 16;
  const LAS char* hp = kv + 1024 + (lane & 31) * 4;
  const LAS char* vp = kv + 1152 + (lane >> 5) * 320 + (lane & 31) * 16;
  f32x16 cin;
#pragma unroll
  for (int i = 0; i < 16; ++i) cin[i] = SUBMAX ? negm : 0.f;
  for (int t = t0; t < t1; ++t) {
    const int off = t * KVT;
    const u32x4 ka = lds128(kp + off);
    const u32x4 kb = {*(const LAS unsigned*)(hp + off), 0u, 0u, 0u};
    const u32x4 v0 = lds128(vp + off), v1 = lds128(vp + off + 640);
    f32x16 sc = mfma32(ka, q0, cin);
    sc = mfma32(kb, q1, sc);
    u32x4 p0, p1;
#pragma unroll
    for (int i = 0; i < 4; ++i) {
      p0[i] = cvtpk(__builtin_amdgcn_exp2f(sc[2 * i]), __builtin_amdgcn_exp2f(sc[2 * i + 1]));
      p1[i] = cvtpk(__builtin_amdgcn_exp2f(sc[8 + 2 * i]), __builtin_amdgcn_exp2f(sc[8 + 2 * i + 1]));
    }
    o = mfma32(v0, p0, o);
    o = mfma32(v1, p1, o);
  }
}
__global__ void __launch_bounds__(512, 2) k2_attn(char* __restrict__ ws) {
  __shared__ __attribute__((aligned(16))) char lds[KVH + 256];
  const int tid = threadIdx.x, lane = tid & 63, w = __builtin_amdgcn_readfirstlane(tid >> 6);
  const int xcd = blockIdx.x & 7, idx = blockIdx.x >> 3, h = 4 * xcd + (idx & 3), qb = idx >> 2;
  const int qt = qb * 8 + w;
  const u32x4 q0 = *(const u32x4*)(ws + QLO_OFF + ((size_t)h * 64 + qt) * 1024 + lane * 16);
  const unsigned qh = *(const unsigned*)(ws + QHI_OFF + ((size_t)h * 2048 + qt * 32 + (lane & 31)) * 4);
  const char* src = ws + KV_OFF + (size_t)h * KVH + w * 1024 + lane * 16;
  const unsigned lb = (unsigned)(size_t)(LAS char*)lds + w * 1024;
#pragma unroll
  for (int r = 0; r < 19; ++r) glds16(src + r * 8192, lb + r * 8192);
  const u32x4 q1 = {lane < 32 ? qh : 0u, 0u, 0u, 0u};
  const LAS char* kv = (const LAS char*)lds;
  f32x16 o;
#pragma unroll
  for (int i = 0; i < 16; ++i) o[i] = 0.f;
  asm volatile("s_waitcnt vmcnt(16)\n\ts_barrier" ::: "memory");
  attn_tiles<false>(kv, 0, 10, q0, q1, 0.f, lane, o);
  asm volatile("s_waitcnt vmcnt(12)\n\ts_barrier" ::: "memory");
  attn_tiles<false>(kv, 10, 23, q0, q1, 0.f, lane, o);
  asm volatile("s_waitcnt vmcnt(6)\n\ts_barrier" ::: "memory");
  attn_tiles<false>(kv, 23, 43, q0, q1, 0.f, lane, o);
  asm volatile("s_waitcnt vmcnt(0)\n\ts_barrier" ::: "memory");
  attn_tiles<false>(kv, 43, 64, q0, q1, 0.f, lane, o);
  float l = __shfl(o[10], lane & 31);
  bool bad = !(l >= 7.8886091e-31f && l <= 1.3292280e36f);
#pragma unroll
  for (int i = 0; i < 10; ++i) bad |= !(__builtin_fabsf(o[i]) <= 3.0e38f) && (i < 8 || lane < 32);
  if (__builtin_expect(__any(bad), 0)) {
    float m = -3.0e38f;
    const LAS char* kp = kv + lane * 16;
    const LAS char* hp = kv + 1024 + (lane & 31) * 4;
    f32x16 z;
#pragma unroll
    for (int i = 0; i < 16; ++i) z[i] = 0.f;
    for (int t = 0; t < 64; ++t) {
      const u32x4 ka = lds128(kp + t * KVT);
      const u32x4 kb = {*(const LAS unsigned*)(hp + t * KVT), 0u, 0u, 0u};
      f32x16 sc = mfma32(ka, q0, z);
      sc = mfma32(kb, q1, sc);
#pragma unroll
      for (int i = 0; i < 16; ++i) m = __builtin_fmaxf(m, sc[i]);
    }
    m = __builtin_fmaxf(m, __shfl_xor(m, 32));
#pragma unroll
    for (int i = 0; i < 16; ++i) o[i] = 0.f;
    attn_tiles<true>(kv, 0, 64, q0, q1, -m, lane, o);
    l = __shfl(o[10], lane & 31);
  }
  const float rl = 1.f / l;
  const int q = qt * 32 + (lane & 31), hf = lane >> 5;
  char* ob = ws + OLO_OFF + ((size_t)h * 2048 + q) * 32 + hf * 8;
  *(u32x2*)(ob) = u32x2{cvtpk(o[0] * rl, o[1] * rl), cvtpk(o[2] * rl, o[3] * rl)};
  *(u32x2*)(ob + 16) = u32x2{cvtpk(o[4] * rl, o[5] * rl), cvtpk(o[6] * rl, o[7] * rl)};
  if (lane < 32) *(unsigned*)(ws + OHI_OFF + ((size_t)(h >> 2) * 2048 + q) * 16 + (h & 3) * 4) = cvtpk(o[8] * rl, o[9] * rl);
}

__global__ void __launch_bounds__(576) k3_wo(const u16* __restrict__ x, char* __restrict__ ws) {
  __shared__ __attribute__((aligned(16))) char lds[36 * 1024];
  const int tid = threadIdx.x, lane = tid & 63, w = __builtin_amdgcn_readfirstlane(tid >> 6);
  const int cg = blockIdx.x & 3, rb = blockIdx.x >> 2;
  const int g = lane >> 4, c = lane & 15;
  const unsigned lb = (unsigned)(size_t)(LAS char*)lds;
#pragma unroll
  for (int i = 0; i < 4; ++i) {
    const int fi = w * 4 + i, s = fi >> 1, tt = fi & 1;
    const int ci = 4 * s + g, G = ci / 9, r9 = ci % 9, tok = rb * 32 + tt * 16 + c;
    const char* src = r9 < 8 ? ws + OLO_OFF + ((size_t)(4 * G + (r9 >> 1)) * 2048 + tok) * 32 + (r9 & 1) * 16
                             : ws + OHI_OFF + ((size_t)G * 2048 + tok) * 16;
    glds16(src, lb + fi * 1024);
  }
  const int T = cg * 9 + w;
  const u32x4* ap = (const u32x4*)(ws + WOT_OFF + (size_t)T * 18 * 1024) + lane;
  u32x4 a[18];
#pragma unroll
  for (int s = 0; s < 18; ++s) a[s] = ap[s * 64];
  u32x2 xr[2];
  float ga[4];
#pragma unroll
  for (int t = 0; t < 2; ++t) xr[t] = *(const u32x2*)(x + (size_t)(rb * 32 + t * 16 + c) * D + T * 16 + 4 * g);
#pragma unroll
  for (int j = 0; j < 4; ++j) ga[j] = mod_val((const float*)(ws + MODP_OFF), 2 * D + T * 16 + 4 * g + j);
  SBAR();
  WAIT_VM(0);
  __syncthreads();
  f32x4 acc[2] = {f32x4{0.f, 0.f, 0.f, 0.f}, f32x4{0.f, 0.f, 0.f, 0.f}};
  const LAS char* bp = (const LAS char*)lds + lane * 16;
#pragma unroll
  for (int s = 0; s < 18; ++s)
#pragma unroll
    for (int t = 0; t < 2; ++t) acc[t] = mfma16(a[s], lds128(bp + (s * 2 + t) * 1024), acc[t]);
#pragma unroll
  for (int t = 0; t < 2; ++t) {
    u32x2 o = {cvtpk(bflo(xr[t][0]) + ga[0] * acc[t][0], bfhi(xr[t][0]) + ga[1] * acc[t][1]),
               cvtpk(bflo(xr[t][1]) + ga[2] * acc[t][2], bfhi(xr[t][1]) + ga[3] * acc[t][3])};
    *(u32x2*)(ws + X1_OFF + ((size_t)(rb * 32 + t * 16 + c) * D + T * 16 + 4 * g) * 2) = o;
  }
}

template <int NT, bool FIRST>
DI void mlp_gemm(const u32x4* __restrict__ ap, const LAS char* hb, int lane, f32x4 (&acc)[5][4]) {
  constexpr int NF = NT * KS, WIN = 2 * NT;
  u32x4 a[WIN];
#pragma unroll
  for (int i = 0; i < WIN; ++i) a[i] = ap[i * 64];
#pragma unroll
  for (int j = 0; j < NT; ++j)
#pragma unroll
    for (int t = 0; t < 4; ++t) acc[j][t] = f32x4{0.f, 0.f, 0.f, 0.f};
  const LAS char* b0 = bfrag_base(hb, lane, 0);
  const LAS char* b1 = bfrag_base(hb, lane, 1);
  SBAR();
#pragma unroll
  for (int s = 0; s < KS; ++s) {
    u32x4 bf[4];
#pragma unroll
    for (int t = 0; t < 4; ++t) bf[t] = lds128(((s & 1) ? b1 : b0) + (s * 4 + t) * 1024);
#pragma unroll
    for (int j = 0; j < NT; ++j) {
      const int i = s * NT + j;
      const u32x4 cur = a[i % WIN];
      if (i + WIN < NF) a[i % WIN] = ap[(i + WIN) * 64];
#pragma unroll
      for (int t = 0; t < 4; ++t) acc[j][t] = mfma16(cur, bf[t], acc[j][t]);
    }
    SBAR();
  }
}
template <int NT>
DI void mlp_body(char* __restrict__ ws, LAS char* hb, int w, int lane, int fr, int rb) {
  const int T0 = NT == 5 ? 5 * w : 20 + 4 * (w - 4);
  const size_t wbase = (size_t)fr * 648 + (NT == 5 ? w * 90 : 360 + (w - 4) * 72);
  f32x4 acc[5][4];
  mlp_gemm<NT, true>((const u32x4*)(ws + W1T_OFF + wbase * 1024) + lane, hb, lane, acc);
  __syncthreads();
  const int g = lane >> 4, c = lane & 15;
#pragma unroll
  for (int j = 0; j < NT; ++j) {
    const int T = T0 + j, s = T >> 1, half = T & 1;
#pragma unroll
    for (int t = 0; t < 4; ++t) {
      const f32x4 v = acc[j][t];
      u32x2 o = {cvtpk(silu_f(v[0]), silu_f(v[1])), cvtpk(silu_f(v[2]), silu_f(v[3]))};
      *(LAS u32x2*)(hb + ((s * 4 + t) * 64 + g * 16 + (c ^ ((4 * s + g) & 7))) * 16 + half * 8) = o;
    }
  }
  __syncthreads();
  mlp_gemm<NT, false>((const u32x4*)(ws + W2T_OFF + wbase * 1024) + lane, hb, lane, acc);
#pragma unroll
  for (int j = 0; j < NT; ++j) {
    const int T = T0 + j;
#pragma unroll
    for (int tp = 0; tp < 2; ++tp) {
      const f32x4 v0 = acc[j][2 * tp], v1 = acc[j][2 * tp + 1];
      u32x4 o = {cvtpk(v0[0], v0[1]), cvtpk(v0[2], v0[3]), cvtpk(v1[0], v1[1]), cvtpk(v1[2], v1[3])};
      *(u32x4*)(ws + PART_OFF + ((((size_t)fr * 32 + rb) * 36 + T) * 2 + tp) * 1024 + lane * 16) = o;
    }
  }
}
__global__ void __launch_bounds__(512, 2) k4_mlp(char* __restrict__ ws) {
  __shared__ __attribute__((aligned(16))) char lds[73728 + 4608];
  const int tid = threadIdx.x, lane = tid & 63, w = __builtin_amdgcn_readfirstlane(tid >> 6);
  const int fr = blockIdx.x & 7, rb = blockIdx.x >> 3;
  LAS char* hb = (LAS char*)lds;
  LAS float* modv = (LAS float*)(hb + 73728);
  u32x4 xv[9];
  ln_load((const u16*)(ws + X1_OFF), rb * 64, w, lane, xv);
  mod_to_lds((const float*)(ws + MODP_OFF), 3, modv, tid, 512);
  __syncthreads();
  ln_finish(xv, modv, hb, w, lane);
  __syncthreads();
  if (w < 4) mlp_body<5>(ws, hb, w, lane, fr, rb);
  else mlp_body<4>(ws, hb, w, lane, fr, rb);
}

__global__ void __launch_bounds__(576) k5_out(const char* __restrict__ ws, u16* __restrict__ out) {
  const int tid = threadIdx.x, lane = tid & 63, w = tid >> 6;
  const int rb = blockIdx.x >> 3, sub = blockIdx.x & 7;
  const int unit = sub * 9 + w, T = unit >> 1, tp = unit & 1;
  const int g = lane >> 4, c = lane & 15;
  u32x4 p[8];
#pragma unroll
  for (int f = 0; f < 8; ++f) p[f] = *(const u32x4*)(ws + PART_OFF + ((((size_t)f * 32 + rb) * 36 + T) * 2 + tp) * 1024 + lane * 16);
  const int col = T * 16 + 4 * g;
  u32x2 xr[2];
  float gb[4];
#pragma unroll
  for (int t = 0; t < 2; ++t) xr[t] = *(const u32x2*)(ws + X1_OFF + ((size_t)(rb * 64 + tp * 32 + t * 16 + c) * D + col) * 2);
#pragma unroll
  for (int j = 0; j < 4; ++j) gb[j] = mod_val((const float*)(ws + MODP_OFF), 5 * D + col + j);
#pragma unroll
  for (int t = 0; t < 2; ++t) {
    float a[4] = {0.f, 0.f, 0.f, 0.f};
#pragma unroll
    for (int f = 0; f < 8; ++f) {
      a[0] += bflo(p[f][2 * t]); a[1] += bfhi(p[f][2 * t]);
      a[2] += bflo(p[f][2 * t + 1]); a[3] += bfhi(p[f][2 * t + 1]);
    }
    u32x2 o = {cvtpk(bflo(xr[t][0]) + gb[0] * a[0], bfhi(xr[t][0]) + gb[1] * a[1]),
               cvtpk(bflo(xr[t][1]) + gb[2] * a[2], bfhi(xr[t][1]) + gb[3] * a[3])};
    *(u32x2*)(out + (size_t)(rb * 64 + tp * 32 + t * 16 + c) * D + col) = o;
  }
}

extern "C" void kernel_launch(void* const* d_in, const int* in_sizes, int n_in, void* d_out, int out_size, void* d_ws,
                              size_t ws_size, hipStream_t stream) {
  const u16* x = (const u16*)d_in[0];
  const u16* wqkv = (const u16*)d_in[1];
  const u16* wo = (const u16*)d_in[2];
  const u16* w1 = (const u16*)d_in[4];
  const u16* w2 = (const u16*)d_in[5];
  const u16* c = (const u16*)d_in[6];
  const u16* wmod = (const u16*)d_in[7];
  char* ws = (char*)d_ws;
  k0_prep<<<K0_MOD + K0_TILE_BLKS + K0_QKV_BLKS, 256, 0, stream>>>(c, wmod, wqkv, wo, w1, w2, ws);
  k1_qkv<<<256, 512, 0, stream>>>(x, ws);
  k2_attn<<<256, 512, 0, stream>>>(ws);
  k3_wo<<<256, 576, 0, stream>>>(x, ws);
  k4_mlp<<<256, 512, 0, stream>>>(ws);
  k5_out<<<256, 576, 0, stream>>>(ws, (u16*)d_out);
}
```

```cpp
#include <hip/hip_runtime.h>
#include <hip/hip_bf16.h>
#include <stdint.h>

typedef unsigned short u16;
typedef short bf16x8 __attribute__((ext_vector_type(8)));
typedef short s16x4 __attribute__((ext_vector_type(4)));
typedef float f32x4 __attribute__((ext_vector_type(4)));
typedef float f32x16 __attribute__((ext_vector_type(16)));
typedef unsigned u32x4 __attribute__((ext_vector_type(4)));
typedef unsigned u32x2 __attribute__((ext_vector_type(2)));
#define LAS __attribute__((address_space(3)))
#define GAS __attribute__((address_space(1)))
#define DI __device__ __forceinline__

constexpr int S = 2048, D = 576, NH = 32, DH = 18, F = 4608;
constexpr int KS = 18;

constexpr size_t MODP_OFF = 0;
constexpr size_t WQKVT_OFF = 0x10000;
constexpr size_t WOT_OFF = WQKVT_OFF + 8 * 14 * 18 * 1024;
constexpr size_t W1T_OFF = WOT_OFF + 36 * 18 * 1024;
constexpr size_t W2T_OFF = W1T_OFF + 8 * 648 * 1024;
constexpr size_t QLO_OFF = W2T_OFF + 8 * 648 * 1024;
constexpr size_t QHI_OFF = QLO_OFF + 32 * 64 * 1024;
constexpr int KVT = 2432;
constexpr int KVH = 64 * KVT;
constexpr size_t KV_OFF = QHI_OFF + 32 * 2048 * 4;
constexpr size_t OLO_OFF = KV_OFF + (size_t)32 * KVH;
constexpr size_t OHI_OFF = OLO_OFF + 32 * 2048 * 32;
constexpr size_t X1_OFF = OHI_OFF + 8 * 2048 * 16;
constexpr size_t PART_OFF = X1_OFF + (size_t)S * D * 2;
constexpr size_t WS_END = PART_OFF + (size_t)8 * S * D * 2;

#define TS(kid, i)
DI float bf2f(unsigned v) { return __builtin_bit_cast(float, v << 16); }
DI float bflo(unsigned v) { return __builtin_bit_cast(float, v << 16); }
DI float bfhi(unsigned v) { return __builtin_bit_cast(float, v & 0xffff0000u); }
typedef __bf16 bf16x2_t __attribute__((ext_vector_type(2)));
typedef float f32x2 __attribute__((ext_vector_type(2)));
DI unsigned cvtpk(float lo, float hi) {
  return __builtin_bit_cast(unsigned, __builtin_convertvector(f32x2{lo, hi}, bf16x2_t));
}
DI float rbf(float x) { return bflo(cvtpk(x, x)); }
DI f32x4 mfma16(u32x4 a, u32x4 b, f32x4 c) {
  return __builtin_amdgcn_mfma_f32_16x16x32_bf16(__builtin_bit_cast(bf16x8, a), __builtin_bit_cast(bf16x8, b), c, 0, 0, 0);
}
DI f32x16 mfma32(u32x4 a, u32x4 b, f32x16 c) {
  return __builtin_amdgcn_mfma_f32_32x32x16_bf16(__builtin_bit_cast(bf16x8, a), __builtin_bit_cast(bf16x8, b), c, 0, 0, 0);
}
DI void glds16(const void* g, unsigned lds_base) {
  unsigned sv;
  asm volatile("s_mov_b32 %0, m0\n\ts_mov_b32 m0, %2\n\ts_nop 0\n\tglobal_load_lds_dwordx4 %1, off\n\ts_mov_b32 m0, %0"
               : "=&s"(sv) : "v"(g), "s"(lds_base) : "memory");
}
#define WAIT_VM(n) asm volatile("s_waitcnt vmcnt(" #n ")" ::: "memory")
#define SBAR() __builtin_amdgcn_sched_barrier(0)
DI float silu_f(float x) { return x * __builtin_amdgcn_rcpf(1.f + __builtin_amdgcn_exp2f(-1.4426950408889634f * x)); }
typedef short v4i16_t __attribute__((ext_vector_type(4)));
DI u32x2 tr_read(const LAS char* p) {
  return __builtin_bit_cast(u32x2, __builtin_amdgcn_ds_read_tr16_b64_v4i16((LAS v4i16_t*)p));
}
DI u32x4 lds128(const LAS char* p) { return *(const LAS u32x4*)p; }

DI int frag_index(int T, int s) {
  return T < 20 ? (T / 5) * 90 + s * 5 + (T % 5) : 360 + ((T - 20) >> 2) * 72 + s * 4 + ((T - 20) & 3);
}

constexpr int K0_MOD = 216, K0_U1 = 1296, K0_U2 = 1296, K0_UO = 162, K0_UNITS = K0_U1 + K0_U2 + K0_UO;
constexpr int K0_TILE_BLKS = (K0_UNITS + 3) / 4, K0_QKV_BLKS = 2016 / 4;
__global__ void __launch_bounds__(256) k0_prep(const u16* __restrict__ c, const u16* __restrict__ wmod, const u16* __restrict__ wqkv,
                                               const u16* __restrict__ wo, const u16* __restrict__ w1, const u16* __restrict__ w2,
                                               char* __restrict__ ws) {
  __shared__ __attribute__((aligned(16))) char lds[16384];
  const int tid = threadIdx.x, lane = tid & 63, w = __builtin_amdgcn_readfirstlane(tid >> 6);
  int b = blockIdx.x;
  TS(0, 0);
  if (b < K0_MOD) {
    const int cc = b % 54, ks = b / 54;
    const int ch = tid & 7, rg = tid >> 3;
    float acc[8];
#pragma unroll
    for (int j = 0; j < 8; ++j) acc[j] = 0.f;
    u32x4 wv[5];
    float sv[5];
#pragma unroll
    for (int i = 0; i < 5; ++i) {
      int r = rg + 32 * i;
      bool ok = r < 144;
      int k = ks * 144 + (ok ? r : 0);
      wv[i] = *(const u32x4*)(wmod + (size_t)k * 3456 + cc * 64 + ch * 8);
      float cv = bf2f(c[k]);
      sv[i] = ok ? rbf(cv / (1.f + __expf(-cv))) : 0.f;
    }
#pragma unroll
    for (int i = 0; i < 5; ++i)
#pragma unroll
      for (int j = 0; j < 4; ++j) {
        acc[2 * j] += sv[i] * bflo(wv[i][j]);
        acc[2 * j + 1] += sv[i] * bfhi(wv[i][j]);
      }
#pragma unroll
    for (int j = 0; j < 8; ++j) {
      acc[j] += __shfl_xor(acc[j], 8);
      acc[j] += __shfl_xor(acc[j], 16);
      acc[j] += __shfl_xor(acc[j], 32);
    }
    float* red = (float*)lds;
    if (lane < 8)
#pragma unroll
      for (int j = 0; j < 8; ++j) red[w * 64 + lane * 8 + j] = acc[j];
    __syncthreads();
    if (tid < 64) ((float*)(ws + MODP_OFF))[ks * 3456 + cc * 64 + tid] = red[tid] + red[64 + tid] + red[128 + tid] + red[192 + tid];
    TS(0, 1);
    return;
  }
  b -= K0_MOD;
  if (b < K0_TILE_BLKS) {
    int u = b * 4 + w;
    if (u >= K0_UNITS) return;
    const u16* src; int ldn, n0, kind, s, fr = 0;
    if (u < K0_U1) { kind = 0; s = u % 18; n0 = (u / 18) * 64; src = w1; ldn = F; }
    else if (u < K0_U1 + K0_U2) { int v = u - K0_U1; kind = 1; int sg = v % 144; fr = sg / 18; s = sg % 18; n0 = (v / 144) * 64; src = w2; ldn = D; }
    else { int v = u - K0_U1 - K0_U2; kind = 2; s = v % 18; n0 = (v / 18) * 64; src = wo; ldn = D; }
    const unsigned lb = (unsigned)(size_t)(LAS char*)lds + w * 4096;
#pragma unroll
    for (int p = 0; p < 4; ++p) {
      int rr = 8 * p + (lane >> 3);
      int g = rr >> 3, j = rr & 7, k;
      if (kind == 0) k = 32 * s + rr;
      else if (kind == 1) k = 576 * fr + 32 * s + 16 * (j >> 2) + 4 * g + (j & 3);
      else { int ci = 4 * s + g, G = ci / 9, r9 = ci % 9; k = r9 < 8 ? (4 * G + (r9 >> 1)) * 18 + 8 * (r9 & 1) + j : (4 * G + (j >> 1)) * 18 + 16 + (j & 1); }
      glds16(src + (size_t)k * ldn + n0 + (lane & 7) * 8, lb + p * 1024);
    }
    WAIT_VM(0);
    const LAS char* img = (const LAS char*)lds + w * 4096;
    const int g = lane >> 4, q4 = (lane & 15) >> 2, p4 = lane & 3;
#pragma unroll
    for (int i = 0; i < 4; ++i) {
      u32x2 lo = tr_read(img + (8 * g + q4) * 128 + i * 32 + p4 * 8);
      u32x2 hi = tr_read(img + (8 * g + 4 + q4) * 128 + i * 32 + p4 * 8);
      u32x4 fragv = {lo[0], lo[1], hi[0], hi[1]};
      int Tn = n0 / 16 + i;
      size_t off;
      if (kind == 0) off = W1T_OFF + ((size_t)(Tn / 36) * 648 + frag_index(Tn % 36, s)) * 1024;
      else if (kind == 1) off = W2T_OFF + ((size_t)fr * 648 + frag_index(Tn, s)) * 1024;
      else off = WOT_OFF + ((size_t)Tn * 18 + s) * 1024;
      *(u32x4*)(ws + off + lane * 16) = fragv;
    }
    TS(0, 1);
    return;
  }
  b -= K0_TILE_BLKS;
  {
    int u = b * 4 + w;
    int s = u % 18, tile = (u / 18) % 14, hq = u / (18 * 14);
    int g = lane >> 4, r = lane & 15;
    int col; bool ok = true;
    if (tile < 12) col = (tile >> 2) * 576 + (4 * hq + (tile & 3)) * 18 + r;
    else if (tile == 12) { int i = r >> 1; col = (i >> 2) * 576 + (4 * hq + (i & 3)) * 18 + 16 + (r & 1); }
    else { ok = r < 8; int i = (r >> 1) & 3; col = 1152 + (4 * hq + i) * 18 + 16 + (r & 1); }
    u16 v[8];
#pragma unroll
    for (int j = 0; j < 8; ++j) v[j] = wqkv[(size_t)(32 * s + 8 * g + j) * 1728 + col];
    u32x4 o;
#pragma unroll
    for (int j = 0; j < 4; ++j) o[j] = ok ? ((unsigned)v[2 * j] | ((unsigned)v[2 * j + 1] << 16)) : 0u;
    *(u32x4*)(ws + WQKVT_OFF + (size_t)u * 1024 + lane * 16) = o;
    TS(0, 1);
  }
}

DI void ln_load(const u16* __restrict__ x, int row0, int w, int lane, u32x4 (&xv)[9]) {
  const u16* p = x + (size_t)(row0 + 8 * w + (lane >> 3)) * D + (lane & 7) * 8;
#pragma unroll
  for (int i = 0; i < 9; ++i) xv[i] = *(const u32x4*)(p + 64 * i);
}
DI void ln_finish(const u32x4 (&xv)[9], const LAS float* modv, LAS char* hb, int w, int lane) {
  float s = 0.f;
#pragma unroll
  for (int i = 0; i < 9; ++i)
#pragma unroll
    for (int j = 0; j < 4; ++j) s += bflo(xv[i][j]) + bfhi(xv[i][j]);
  s += __shfl_xor(s, 1); s += __shfl_xor(s, 2); s += __shfl_xor(s, 4);
  const float mean = s * (1.f / D);
  float q = 0.f;
#pragma unroll
  for (int i = 0; i < 9; ++i)
#pragma unroll
    for (int j = 0; j < 4; ++j) { float a = bflo(xv[i][j]) - mean, b = bfhi(xv[i][j]) - mean; q += a * a; q += b * b; }
  q += __shfl_xor(q, 1); q += __shfl_xor(q, 2); q += __shfl_xor(q, 4);
  const float rstd = __builtin_amdgcn_rsqf(q * (1.f / D) + 1e-5f);
  const float nb = -mean * rstd;
  const int tk = 8 * w + (lane >> 3), tt = tk >> 4, c = tk & 15;
#pragma unroll
  for (int i = 0; i < 9; ++i) {
    const int ch = (lane & 7) + 8 * i, sidx = ch >> 2, g = ch & 3;
    const LAS f32x4* gp = (const LAS f32x4*)(modv + ch * 8);
    const LAS f32x4* sp = (const LAS f32x4*)(modv + 576 + ch * 8);
    f32x4 g0 = gp[0], g1 = gp[1], s0 = sp[0], s1 = sp[1];
    u32x4 o;
    o[0] = cvtpk((bflo(xv[i][0]) * rstd + nb) * g0[0] + s0[0], (bfhi(xv[i][0]) * rstd + nb) * g0[1] + s0[1]);
    o[1] = cvtpk((bflo(xv[i][1]) * rstd + nb) * g0[2] + s0[2], (bfhi(xv[i][1]) * rstd + nb) * g0[3] + s0[3]);
    o[2] = cvtpk((bflo(xv[i][2]) * rstd + nb) * g1[0] + s1[0], (bfhi(xv[i][2]) * rstd + nb) * g1[1] + s1[1]);
    o[3] = cvtpk((bflo(xv[i][3]) * rstd + nb) * g1[2] + s1[2], (bfhi(xv[i][3]) * rstd + nb) * g1[3] + s1[3]);
    *(LAS u32x4*)(hb + ((sidx * 4 + tt) * 64 + g * 16 + (c ^ (ch & 7))) * 16) = o;
  }
}
DI void mod_to_lds(const float* __restrict__ modp, int which, LAS float* modv, int tid, int nthr) {
  for (int i = tid; i < 2 * D; i += nthr) {
    const int isshift = i >= D, k = isshift ? i - D : i;
    const int col = (which + (isshift ? 0 : 1)) * D + k;
    const float v = rbf(modp[col] + modp[3456 + col] + modp[2 * 3456 + col] + modp[3 * 3456 + col]);
    modv[i] = isshift ? v : rbf(1.f + v);
  }
}
DI float mod_val(const float* __restrict__ modp, int col) {
  return rbf(modp[col] + modp[3456 + col] + modp[2 * 3456 + col] + modp[3 * 3456 + col]);
}
DI const LAS char* bfrag_base(const LAS char* hb, int lane, int par) {
  const int g = lane >> 4, c = lane & 15;
  return hb + (g * 16 + (c ^ ((4 * par + g) & 7))) * 16;
}

constexpr float QSCALE = 0.23570226039551584f * 1.4426950408889634f;
__global__ void __launch_bounds__(512, 2) k1_qkv(const u16* __restrict__ x, char* __restrict__ ws) {
  __shared__ __attribute__((aligned(16))) char lds[73728 + 4608];
  const int tid = threadIdx.x, lane = tid & 63, w = __builtin_amdgcn_readfirstlane(tid >> 6);
  const int hq = blockIdx.x & 7, rb = blockIdx.x >> 3;
  LAS char* hb = (LAS char*)lds;
  LAS float* modv = (LAS float*)(hb + 73728);
  u32x4 xv[9];
  TS(1, 0);
  ln_load(x, rb * 64, w, lane, xv);
  mod_to_lds((const float*)(ws + MODP_OFF), 0, modv, tid, 512);
  constexpr int WIN = 16;
  u32x4 a[WIN];
  const int wt = w < 7 ? w : 6;
  const u32x4* ap = (const u32x4*)(ws + WQKVT_OFF + ((size_t)hq * 14 + 2 * wt) * 18 * 1024) + lane;
#pragma unroll
  for (int i = 0; i < WIN; ++i) a[i] = ap[((i & 1) * 18 + (i >> 1)) * 64];
  SBAR();
  __syncthreads();
  TS(1, 1);
  ln_finish(xv, modv, hb, w, lane);
  TS(1, 2);
  __syncthreads();
  TS(1, 3);
  if (w == 7) return;
  f32x4 acc[2][4];
#pragma unroll
  for (int j = 0; j < 2; ++j)
#pragma unroll
    for (int t = 0; t < 4; ++t) acc[j][t] = f32x4{0.f, 0.f, 0.f, 0.f};
  const LAS char* b0 = bfrag_base(hb, lane, 0);
  const LAS char* b1 = bfrag_base(hb, lane, 1);
  const bool n0 = (w == 4 || w == 5), n1 = (w >= 4);
#pragma unroll
  for (int s = 0; s < KS; ++s) {
    u32x4 bf[4];
#pragma unroll
    for (int t = 0; t < 4; ++t) bf[t] = lds128(((s & 1) ? b1 : b0) + (s * 4 + t) * 1024);
#pragma unroll
    for (int j = 0; j < 2; ++j) {
      const int i = 2 * s + j;
      const u32x4 cur = a[i % WIN];
      if (i + WIN < 36) a[i % WIN] = ap[(((i + WIN) & 1) * 18 + ((i + WIN) >> 1)) * 64];
      if (j == 0 ? n0 : n1) {
#pragma unroll
        for (int t = 0; t < 4; ++t) acc[j][t] = mfma16(bf[t], cur, acc[j][t]);
      } else {
#pragma unroll
        for (int t = 0; t < 4; ++t) acc[j][t] = mfma16(cur, bf[t], acc[j][t]);
      }
    }
    SBAR();
  }
  TS(1, 4);
  const int g = lane >> 4, c = lane & 15;
#pragma unroll
  for (int j = 0; j < 2; ++j) {
    const int tile = 2 * w + j;
#pragma unroll
    for (int t = 0; t < 4; ++t) {
      const f32x4 v = acc[j][t];
      if (tile < 8) {
        const int tok = rb * 64 + t * 16 + c, h = 4 * hq + (tile & 3);
        const float sc = tile < 4 ? QSCALE : 1.f;
        u32x2 o = {cvtpk(v[0] * sc, v[1] * sc), cvtpk(v[2] * sc, v[3] * sc)};
        const int sub = ((g >> 1) * 32 + (tok & 31)) * 16 + (g & 1) * 8;
        char* dst = tile < 4 ? ws + QLO_OFF + ((size_t)h * 64 + (tok >> 5)) * 1024 + sub
                             : ws + KV_OFF + (size_t)h * KVH + (tok >> 5) * KVT + sub;
        *(u32x2*)dst = o;
      } else if (tile < 12 || tile == 13) {
        const int tok0 = rb * 64 + t * 16, kt = tok0 >> 5, sstep = (tok0 >> 4) & 1;
        int h, d; u32x2 o = {cvtpk(v[0], v[1]), cvtpk(v[2], v[3])};
        if (tile < 12) { h = 4 * hq + (tile & 3); d = c; }
        else if (c < 8) { h = 4 * hq + (c >> 1); d = 16 + (c & 1); }
        else { h = 4 * hq + (c & 3); d = c < 12 ? 18 : 19; const unsigned cv = c < 12 ? 0x3F803F80u : 0u; o = u32x2{cv, cv}; }
        *(u32x2*)(ws + KV_OFF + (size_t)h * KVH + kt * KVT + 1152 + sstep * 640 + (g & 1) * 320 + d * 16 + (g >> 1) * 8) = o;
      } else {
        const int tok = rb * 64 + t * 16 + c;
        const bool isq = g < 2;
        const float sc = isq ? QSCALE : 1.f;
#pragma unroll
        for (int e = 0; e < 2; ++e) {
          const int h = 4 * hq + 2 * (g & 1) + e;
          const unsigned o = cvtpk(v[2 * e] * sc, v[2 * e + 1] * sc);
          char* dst = isq ? ws + QHI_OFF + ((size_t)h * 2048 + tok) * 4
                          : ws + KV_OFF + (size_t)h * KVH + (tok >> 5) * KVT + 1024 + (tok & 31) * 4;
          *(unsigned*)dst = o;
        }
      }
    }
  }
  TS(1, 5);
}

#define ATT_ORDER 0
DI float ex2(float x) { return __builtin_amdgcn_exp2f(x); }
DI void attn_step(const LAS char* kp, const LAS char* hp, const LAS char* vp, int off, u32x4& ka, u32x4& kb, u32x4& v0, u32x4& v1,
                  const u32x4 q0, const u32x4 q1, const f32x16& cin, const f32x16& sc, f32x16& sn, f32x16& o) {
  u32x4 p0, p1;
  float e0, e1, e2, e3;
  sn = mfma32(ka, q0, cin);
  SBAR();
  ka = lds128(kp + off + 2 * KVT);
  e0 = ex2(sc[0]); e1 = ex2(sc[1]); e2 = ex2(sc[2]); e3 = ex2(sc[3]);
  SBAR();
  p0[0] = cvtpk(e0, e1); p0[1] = cvtpk(e2, e3);
  SBAR();
  sn = mfma32(kb, q1, sn);
  SBAR();
  kb[0] = *(const LAS unsigned*)(hp + off + 2 * KVT);
  e0 = ex2(sc[4]); e1 = ex2(sc[5]); e2 = ex2(sc[6]); e3 = ex2(sc[7]);
  SBAR();
  p0[2] = cvtpk(e0, e1); p0[3] = cvtpk(e2, e3);
  SBAR();
  o = mfma32(v0, p0, o);
  SBAR();
  v0 = lds128(vp + off + KVT);
  e0 = ex2(sc[8]); e1 = ex2(sc[9]); e2 = ex2(sc[10]); e3 = ex2(sc[11]);
  SBAR();
  p1[0] = cvtpk(e0, e1); p1[1] = cvtpk(e2, e3);
  SBAR();
  e0 = ex2(sc[12]); e1 = ex2(sc[13]); e2 = ex2(sc[14]); e3 = ex2(sc[15]);
  SBAR();
  p1[2] = cvtpk(e0, e1); p1[3] = cvtpk(e2, e3);
  SBAR();
  o = mfma32(v1, p1, o);
  SBAR();
  v1 = lds128(vp + off + KVT + 640);
}
template <bool SUBMAX, bool STAGED>
DI void attn_all(const LAS char* kv, u32x4 q0, u32x4 q1, float negm, int lane, f32x16& o) {
  const LAS char* kp = kv + lane * 16;
  const LAS char* hp = kv + 1024 + (lane & 31) * 4;
  const LAS char* vp = kv + 1152 + (lane >> 5) * 320 + (lane & 31) * 16;
  f32x16 cin;
#pragma unroll
  for (int i = 0; i < 16; ++i) cin[i] = SUBMAX ? negm : 0.f;
  u32x4 ka = lds128(kp);
  u32x4 kb = {*(const LAS unsigned*)(hp), 0u, 0u, 0u};
  f32x16 sa = mfma32(ka, q0, cin), sb;
  sa = mfma32(kb, q1, sa);
  ka = lds128(kp + KVT);
  kb[0] = *(const LAS unsigned*)(hp + KVT);
  u32x4 v0 = lds128(vp), v1 = lds128(vp + 640);
#pragma unroll 1
  for (int c = 0; c < 8; ++c) {
    if (STAGED) {
      if (c == 1) asm volatile("s_waitcnt vmcnt(13)\n\ts_barrier" ::: "memory");
      else if (c == 2) asm volatile("s_waitcnt vmcnt(0)\n\ts_barrier" ::: "memory");
    }
#pragma unroll
    for (int u = 0; u < 8; u += 2) {
      attn_step(kp, hp, vp, u * KVT, ka, kb, v0, v1, q0, q1, cin, sa, sb, o);
      attn_step(kp, hp, vp, (u + 1) * KVT, ka, kb, v0, v1, q0, q1, cin, sb, sa, o);
    }
    kp += 8 * KVT; hp += 8 * KVT; vp += 8 * KVT;
  }
}
__global__ void __launch_bounds__(512, 2) k2_attn(char* __restrict__ ws) {
  __shared__ __attribute__((aligned(16))) char lds[163840];
  const int tid = threadIdx.x, lane = tid & 63, w = __builtin_amdgcn_readfirstlane(tid >> 6);
  const int xcd = blockIdx.x & 7, idx = blockIdx.x >> 3, h = 4 * xcd + (idx & 3), qb = idx >> 2;
  const int qt = qb * 8 + w;
  TS(2, 0);
  u32x4 q0 = *(const u32x4*)(ws + QLO_OFF + ((size_t)h * 64 + qt) * 1024 + lane * 16);
  unsigned qh = *(const unsigned*)(ws + QHI_OFF + ((size_t)h * 2048 + qt * 32 + (lane & 31)) * 4);
  const char* src = ws + KV_OFF + (size_t)h * KVH + w * 1024 + lane * 16;
  const unsigned lb = (unsigned)(size_t)(LAS char*)lds + w * 1024;
#pragma unroll
  for (int r = 0; r < 3; ++r) glds16(src + r * 8192, lb + r * 8192);
  asm volatile("" : "+v"(q0), "+v"(qh));
  WAIT_VM(0);
#pragma unroll
  for (int r = 3; r < 19; ++r) glds16(src + r * 8192, lb + r * 8192);
  const u32x4 q1 = {lane < 32 ? qh : 0u, 0u, 0u, 0u};
  const LAS char* kv = (const LAS char*)lds;
  f32x16 o;
#pragma unroll
  for (int i = 0; i < 16; ++i) o[i] = 0.f;
  asm volatile("s_barrier" ::: "memory");
  TS(2, 1);
  attn_all<false, true>(kv, q0, q1, 0.f, lane, o);
  TS(2, 5);
  float l = __shfl(o[10], lane & 31);
  bool bad = !(l >= 7.8886091e-31f && l <= 1.3292280e36f);
#pragma unroll
  for (int i = 0; i < 10; ++i) bad |= !(__builtin_fabsf(o[i]) <= 3.0e38f) && (i < 8 || lane < 32);
  if (__builtin_expect(__any(bad), 0)) {
    float m = -3.0e38f;
    const LAS char* kp = kv + lane * 16;
    const LAS char* hp = kv + 1024 + (lane & 31) * 4;
    f32x16 z;
#pragma unroll
    for (int i = 0; i < 16; ++i) z[i] = 0.f;
#pragma unroll 1
    for (int t = 0; t < 64; ++t) {
      const u32x4 ka = lds128(kp + t * KVT);
      const u32x4 kb = {*(const LAS unsigned*)(hp + t * KVT), 0u, 0u, 0u};
      f32x16 sc = mfma32(ka, q0, z);
      sc = mfma32(kb, q1, sc);
#pragma unroll
      for (int i = 0; i < 16; ++i) m = __builtin_fmaxf(m, sc[i]);
    }
    m = __builtin_fmaxf(m, __shfl_xor(m, 32));
#pragma unroll
    for (int i = 0; i < 16; ++i) o[i] = 0.f;
    attn_all<true, false>(kv, q0, q1, -m, lane, o);
    l = __shfl(o[10], lane & 31);
  }
  const float rl = 1.f / l;
  const int q = qt * 32 + (lane & 31), hf = lane >> 5;
  char* ob = ws + OLO_OFF + ((size_t)h * 2048 + q) * 32 + hf * 8;
  *(u32x2*)(ob) = u32x2{cvtpk(o[0] * rl, o[1] * rl), cvtpk(o[2] * rl, o[3] * rl)};
  *(u32x2*)(ob + 16) = u32x2{cvtpk(o[4] * rl, o[5] * rl), cvtpk(o[6] * rl, o[7] * rl)};
  if (lane < 32) *(unsigned*)(ws + OHI_OFF + ((size_t)(h >> 2) * 2048 + q) * 16 + (h & 3) * 4) = cvtpk(o[8] * rl, o[9] * rl);
  TS(2, 6);
}

__global__ void __launch_bounds__(576) k3_wo(const u16* __restrict__ x, char* __restrict__ ws) {
  __shared__ __attribute__((aligned(16))) char lds[36 * 1024];
  const int tid = threadIdx.x, lane = tid & 63, w = __builtin_amdgcn_readfirstlane(tid >> 6);
  const int cg = blockIdx.x & 3, rb = blockIdx.x >> 2;
  TS(3, 0);
  const int g = lane >> 4, c = lane & 15;
  const unsigned lb = (unsigned)(size_t)(LAS char*)lds;
#pragma unroll
  for (int i = 0; i < 4; ++i) {
    const int fi = w * 4 + i, s = fi >> 1, tt = fi & 1;
    const int ci = 4 * s + g, G = ci / 9, r9 = ci % 9, tok = rb * 32 + tt * 16 + c;
    const char* src = r9 < 8 ? ws + OLO_OFF + ((size_t)(4 * G + (r9 >> 1)) * 2048 + tok) * 32 + (r9 & 1) * 16
                             : ws + OHI_OFF + ((size_t)G * 2048 + tok) * 16;
    glds16(src, lb + fi * 1024);
  }
  const int T = cg * 9 + w;
  const u32x4* ap = (const u32x4*)(ws + WOT_OFF + (size_t)T * 18 * 1024) + lane;
  u32x4 a[18];
#pragma unroll
  for (int s = 0; s < 18; ++s) a[s] = ap[s * 64];
  u32x2 xr[2];
  float ga[4];
#pragma unroll
  for (int t = 0; t < 2; ++t) xr[t] = *(const u32x2*)(x + (size_t)(rb * 32 + t * 16 + c) * D + T * 16 + 4 * g);
#pragma unroll
  for (int j = 0; j < 4; ++j) ga[j] = mod_val((const float*)(ws + MODP_OFF), 2 * D + T * 16 + 4 * g + j);
  SBAR();
  WAIT_VM(0);
  __syncthreads();
  TS(3, 1);
  f32x4 acc[2] = {f32x4{0.f, 0.f, 0.f, 0.f}, f32x4{0.f, 0.f, 0.f, 0.f}};
  const LAS char* bp = (const LAS char*)lds + lane * 16;
#pragma unroll
  for (int s = 0; s < 18; ++s)
#pragma unroll
    for (int t = 0; t < 2; ++t) acc[t] = mfma16(a[s], lds128(bp + (s * 2 + t) * 1024), acc[t]);
#pragma unroll
  for (int t = 0; t < 2; ++t) {
    u32x2 o = {cvtpk(bflo(xr[t][0]) + ga[0] * acc[t][0], bfhi(xr[t][0]) + ga[1] * acc[t][1]),
               cvtpk(bflo(xr[t][1]) + ga[2] * acc[t][2], bfhi(xr[t][1]) + ga[3] * acc[t][3])};
    *(u32x2*)(ws + X1_OFF + ((size_t)(rb * 32 + t * 16 + c) * D + T * 16 + 4 * g) * 2) = o;
  }
  TS(3, 2);
}

template <int NT, bool FIRST>
DI void mlp_gemm(const u32x4* __restrict__ ap, const LAS char* hb, int lane, f32x4 (&acc)[5][4]) {
  constexpr int NF = NT * KS, WIN = 2 * NT;
  u32x4 a[WIN];
#pragma unroll
  for (int i = 0; i < WIN; ++i) a[i] = ap[i * 64];
#pragma unroll
  for (int j = 0; j < NT; ++j)
#pragma unroll
    for (int t = 0; t < 4; ++t) acc[j][t] = f32x4{0.f, 0.f, 0.f, 0.f};
  const LAS char* b0 = bfrag_base(hb, lane, 0);
  const LAS char* b1 = bfrag_base(hb, lane, 1);
  SBAR();
#pragma unroll
  for (int s = 0; s < KS; ++s) {
    u32x4 bf[4];
#pragma unroll
    for (int t = 0; t < 4; ++t) bf[t] = lds128(((s & 1) ? b1 : b0) + (s * 4 + t) * 1024);
#pragma unroll
    for (int j = 0; j < NT; ++j) {
      const int i = s * NT + j;
      const u32x4 cur = a[i % WIN];
      if (i + WIN < NF) a[i % WIN] = ap[(i + WIN) * 64];
#pragma unroll
      for (int t = 0; t < 4; ++t) acc[j][t] = mfma16(cur, bf[t], acc[j][t]);
    }
    SBAR();
  }
}
template <int NT>
DI void mlp_body(char* __restrict__ ws, LAS char* hb, int w, int lane, int fr, int rb) {
  const int T0 = NT == 5 ? 5 * w : 20 + 4 * (w - 4);
  const size_t wbase = (size_t)fr * 648 + (NT == 5 ? w * 90 : 360 + (w - 4) * 72);
  f32x4 acc[5][4];
  mlp_gemm<NT, true>((const u32x4*)(ws + W1T_OFF + wbase * 1024) + lane, hb, lane, acc);
  TS(4, 3);
  __syncthreads();
  TS(4, 4);
  const int g = lane >> 4, c = lane & 15;
#pragma unroll
  for (int j = 0; j < NT; ++j) {
    const int T = T0 + j, s = T >> 1, half = T & 1;
#pragma unroll
    for (int t = 0; t < 4; ++t) {
      const f32x4 v = acc[j][t];
      u32x2 o = {cvtpk(silu_f(v[0]), silu_f(v[1])), cvtpk(silu_f(v[2]), silu_f(v[3]))};
      *(LAS u32x2*)(hb + ((s * 4 + t) * 64 + g * 16 + (c ^ ((4 * s + g) & 7))) * 16 + half * 8) = o;
    }
  }
  TS(4, 5);
  __syncthreads();
  TS(4, 6);
  mlp_gemm<NT, false>((const u32x4*)(ws + W2T_OFF + wbase * 1024) + lane, hb, lane, acc);
  TS(4, 7);
#pragma unroll
  for (int j = 0; j < NT; ++j) {
    const int T = T0 + j;
#pragma unroll
    for (int tp = 0; tp < 2; ++tp) {
      const f32x4 v0 = acc[j][2 * tp], v1 = acc[j][2 * tp + 1];
      u32x4 o = {cvtpk(v0[0], v0[1]), cvtpk(v0[2], v0[3]), cvtpk(v1[0], v1[1]), cvtpk(v1[2], v1[3])};
      *(u32x4*)(ws + PART_OFF + ((((size_t)fr * 32 + rb) * 36 + T) * 2 + tp) * 1024 + lane * 16) = o;
    }
  }
  TS(4, 8);
}
__global__ void __launch_bounds__(512, 2) k4_mlp(char* __restrict__ ws) {
  __shared__ __attribute__((aligned(16))) char lds[73728 + 4608];
  const int tid = threadIdx.x, lane = tid & 63, w = __builtin_amdgcn_readfirstlane(tid >> 6);
  const int fr = blockIdx.x & 7, rb = blockIdx.x >> 3;
  LAS char* hb = (LAS char*)lds;
  LAS float* modv = (LAS float*)(hb + 73728);
  u32x4 xv[9];
  TS(4, 0);
  ln_load((const u16*)(ws + X1_OFF), rb * 64, w, lane, xv);
  mod_to_lds((const float*)(ws + MODP_OFF), 3, modv, tid, 512);
  __syncthreads();
  TS(4, 1);
  ln_finish(xv, modv, hb, w, lane);
  __syncthreads();
  TS(4, 2);
  if (w < 4) mlp_body<5>(ws, hb, w, lane, fr, rb);
  else mlp_body<4>(ws, hb, w, lane, fr, rb);
}

__global__ void __launch_bounds__(576) k5_out(char* __restrict__ ws, u16* __restrict__ out) {
  const int tid = threadIdx.x, lane = tid & 63, w = tid >> 6;
  TS(5, 0);
  const int rb = blockIdx.x >> 3, sub = blockIdx.x & 7;
  const int unit = sub * 9 + w, T = unit >> 1, tp = unit & 1;
  const int g = lane >> 4, c = lane & 15;
  u32x4 p[8];
#pragma unroll
  for (int f = 0; f < 8; ++f) p[f] = *(const u32x4*)(ws + PART_OFF + ((((size_t)f * 32 + rb) * 36 + T) * 2 + tp) * 1024 + lane * 16);
  const int col = T * 16 + 4 * g;
  u32x2 xr[2];
  float gb[4];
#pragma unroll
  for (int t = 0; t < 2; ++t) xr[t] = *(const u32x2*)(ws + X1_OFF + ((size_t)(rb * 64 + tp * 32 + t * 16 + c) * D + col) * 2);
#pragma unroll
  for (int j = 0; j < 4; ++j) gb[j] = mod_val((const float*)(ws + MODP_OFF), 5 * D + col + j);
#pragma unroll
  for (int t = 0; t < 2; ++t) {
    float a[4] = {0.f, 0.f, 0.f, 0.f};
#pragma unroll
    for (int f = 0; f < 8; ++f) {
      a[0] += bflo(p[f][2 * t]); a[1] += bfhi(p[f][2 * t]);
      a[2] += bflo(p[f][2 * t + 1]); a[3] += bfhi(p[f][2 * t + 1]);
    }
    u32x2 o = {cvtpk(bflo(xr[t][0]) + gb[0] * a[0], bfhi(xr[t][0]) + gb[1] * a[1]),
               cvtpk(bflo(xr[t][1]) + gb[2] * a[2], bfhi(xr[t][1]) + gb[3] * a[3])};
    *(u32x2*)(out + (size_t)(rb * 64 + tp * 32 + t * 16 + c) * D + col) = o;
  }
  TS(5, 1);
}

extern "C" void kernel_launch(void* const* d_in, const int* in_sizes, int n_in, void* d_out, int out_size, void* d_ws,
                              size_t ws_size, hipStream_t stream) {
  const u16* x = (const u16*)d_in[0];
  const u16* wqkv = (const u16*)d_in[1];
  const u16* wo = (const u16*)d_in[2];
  const u16* w1 = (const u16*)d_in[4];
  const u16* w2 = (const u16*)d_in[5];
  const u16* c = (const u16*)d_in[6];
  const u16* wmod = (const u16*)d_in[7];
  char* ws = (char*)d_ws;
  k0_prep<<<K0_MOD + K0_TILE_BLKS + K0_QKV_BLKS, 256, 0, stream>>>(c, wmod, wqkv, wo, w1, w2, ws);
  k1_qkv<<<256, 512, 0, stream>>>(x, ws);
  k2_attn<<<256, 512, 0, stream>>>(ws);
  k3_wo<<<256, 576, 0, stream>>>(x, ws);
  k4_mlp<<<256, 512, 0, stream>>>(ws);
  k5_out<<<256, 576, 0, stream>>>(ws, (u16*)d_out);
}
```

```cpp
#include <hip/hip_runtime.h>
#include <hip/hip_bf16.h>
#include <stdint.h>

typedef unsigned short u16;
typedef short bf16x8 __attribute__((ext_vector_type(8)));
typedef short s16x4 __attribute__((ext_vector_type(4)));
typedef float f32x4 __attribute__((ext_vector_type(4)));
typedef float f32x16 __attribute__((ext_vector_type(16)));
typedef unsigned u32x4 __attribute__((ext_vector_type(4)));
typedef unsigned u32x2 __attribute__((ext_vector_type(2)));
#define LAS __attribute__((address_space(3)))
#define GAS __attribute__((address_space(1)))
#define DI __device__ __forceinline__

constexpr int S = 2048, D = 576, NH = 32, DH = 18, F = 4608;
constexpr int KS = 18;

constexpr size_t MODP_OFF = 0;
constexpr size_t WQKVT_OFF = 0x10000;
constexpr size_t WOT_OFF = WQKVT_OFF + 8 * 14 * 18 * 1024;
constexpr size_t W1T_OFF = WOT_OFF + 36 * 18 * 1024;
constexpr size_t W2T_OFF = W1T_OFF + 8 * 648 * 1024;
constexpr size_t QLO_OFF = W2T_OFF + 8 * 648 * 1024;
constexpr size_t QHI_OFF = QLO_OFF + 32 * 64 * 1024;
constexpr int KVT = 2432;
constexpr int KVH = 64 * KVT;
constexpr size_t KV_OFF = QHI_OFF + 32 * 2048 * 4;
constexpr size_t OLO_OFF = KV_OFF + (size_t)32 * KVH;
constexpr size_t OHI_OFF = OLO_OFF + 32 * 2048 * 32;
constexpr size_t X1_OFF = OHI_OFF + 8 * 2048 * 16;
constexpr size_t X1G_OFF = X1_OFF + (size_t)S * D * 2;
constexpr size_t NF_OFF = X1G_OFF + (size_t)S * D * 2;
constexpr size_t STAT_OFF = NF_OFF + (size_t)S * D * 2;
constexpr size_t CSB_OFF = STAT_OFF + 64 * 4 * 64 * 4;
constexpr size_t MODF_OFF = CSB_OFF + 3 * 2 * 4608 * 4;
constexpr size_t PART_OFF = CSB_OFF + 0x20000;
constexpr size_t WS_END = PART_OFF + (size_t)8 * S * D * 2;

#define TSDECL
#define TS(kid, i)
#define TSEND(kid)
#define PROFARG
#define PROFPASS
DI float bf2f(unsigned v) { return __builtin_bit_cast(float, v << 16); }
DI float bflo(unsigned v) { return __builtin_bit_cast(float, v << 16); }
DI float bfhi(unsigned v) { return __builtin_bit_cast(float, v & 0xffff0000u); }
typedef __bf16 bf16x2_t __attribute__((ext_vector_type(2)));
typedef float f32x2 __attribute__((ext_vector_type(2)));
DI unsigned cvtpk(float lo, float hi) {
  return __builtin_bit_cast(unsigned, __builtin_convertvector(f32x2{lo, hi}, bf16x2_t));
}
DI float rbf(float x) { return bflo(cvtpk(x, x)); }
DI f32x4 mfma16(u32x4 a, u32x4 b, f32x4 c) {
  return __builtin_amdgcn_mfma_f32_16x16x32_bf16(__builtin_bit_cast(bf16x8, a), __builtin_bit_cast(bf16x8, b), c, 0, 0, 0);
}
DI f32x16 mfma32(u32x4 a, u32x4 b, f32x16 c) {
  return __builtin_amdgcn_mfma_f32_32x32x16_bf16(__builtin_bit_cast(bf16x8, a), __builtin_bit_cast(bf16x8, b), c, 0, 0, 0);
}
DI void glds16(const void* g, unsigned lds_base) {
  unsigned sv;
  asm volatile("s_mov_b32 %0, m0\n\ts_mov_b32 m0, %2\n\ts_nop 0\n\tglobal_load_lds_dwordx4 %1, off\n\ts_mov_b32 m0, %0"
               : "=&s"(sv) : "v"(g), "s"(lds_base) : "memory");
}
typedef __amdgpu_buffer_rsrc_t rsrc_t;
DI rsrc_t ws_rsrc(const char* ws) { return __builtin_amdgcn_make_buffer_rsrc((void*)ws, (short)0, (int)WS_END, 0x00020000); }
template <int AUX> DI void bst16(rsrc_t r, unsigned voff, unsigned soff, u32x4 v) { __builtin_amdgcn_raw_buffer_store_b128(v, r, voff, soff, AUX); }
template <int AUX> DI void bst8(rsrc_t r, unsigned voff, unsigned soff, u32x2 v) { __builtin_amdgcn_raw_buffer_store_b64(v, r, voff, soff, AUX); }
template <int AUX> DI void bst4(rsrc_t r, unsigned voff, unsigned soff, unsigned v) { __builtin_amdgcn_raw_buffer_store_b32(v, r, voff, soff, AUX); }
DI u32x4 bld16(rsrc_t r, unsigned voff, unsigned soff) { return __builtin_amdgcn_raw_buffer_load_b128(r, voff, soff, 0); }
#define AUX_RT 16
#define AUX_PART 16
#define WAIT_VM(n) asm volatile("s_waitcnt vmcnt(" #n ")" ::: "memory")
#define SBAR() __builtin_amdgcn_sched_barrier(0)
DI float silu_f(float x) { return x * __builtin_amdgcn_rcpf(1.f + __builtin_amdgcn_exp2f(-1.4426950408889634f * x)); }
typedef short v4i16_t __attribute__((ext_vector_type(4)));
DI u32x2 tr_read(const LAS char* p) {
  return __builtin_bit_cast(u32x2, __builtin_amdgcn_ds_read_tr16_b64_v4i16((LAS v4i16_t*)p));
}
DI u32x4 lds128(const LAS char* p) { return *(const LAS u32x4*)p; }

DI int frag_index(int T, int s) {
  return T < 20 ? (T / 5) * 90 + s * 5 + (T % 5) : 360 + ((T - 20) >> 2) * 72 + s * 4 + ((T - 20) & 3);
}

constexpr int K0_MOD = 216, K0_LN = 128, K0_QKV_BLKS = 2016 / 4;
__global__ void __launch_bounds__(256) k0_prep(const u16* __restrict__ c, const u16* __restrict__ wmod, const u16* __restrict__ wqkv,
                                               const u16* __restrict__ x, char* __restrict__ ws) {
  __shared__ __attribute__((aligned(16))) char lds[1024];
  const int tid = threadIdx.x, lane = tid & 63, w = __builtin_amdgcn_readfirstlane(tid >> 6);
  int b = blockIdx.x;
  TSDECL; TS(0, 0);
  if (b < K0_MOD) {
    const int cc = b % 54, ks = b / 54;
    const int ch = tid & 7, rg = tid >> 3;
    float acc[8];
#pragma unroll
    for (int j = 0; j < 8; ++j) acc[j] = 0.f;
    u32x4 wv[5];
    float sv[5];
#pragma unroll
    for (int i = 0; i < 5; ++i) {
      int r = rg + 32 * i;
      bool ok = r < 144;
      int k = ks * 144 + (ok ? r : 0);
      wv[i] = *(const u32x4*)(wmod + (size_t)k * 3456 + cc * 64 + ch * 8);
      float cv = bf2f(c[k]);
      sv[i] = ok ? rbf(cv / (1.f + __expf(-cv))) : 0.f;
    }
#pragma unroll
    for (int i = 0; i < 5; ++i)
#pragma unroll
      for (int j = 0; j < 4; ++j) {
        acc[2 * j] += sv[i] * bflo(wv[i][j]);
        acc[2 * j + 1] += sv[i] * bfhi(wv[i][j]);
      }
#pragma unroll
    for (int j = 0; j < 8; ++j) {
      acc[j] += __shfl_xor(acc[j], 8);
      acc[j] += __shfl_xor(acc[j], 16);
      acc[j] += __shfl_xor(acc[j], 32);
    }
    float* red = (float*)lds;
    if (lane < 8)
#pragma unroll
      for (int j = 0; j < 8; ++j) red[w * 64 + lane * 8 + j] = acc[j];
    __syncthreads();
    if (tid < 64) ((float*)(ws + MODP_OFF))[ks * 3456 + cc * 64 + tid] = red[tid] + red[64 + tid] + red[128 + tid] + red[192 + tid];
    TS(0, 1); TSEND(0);
    return;
  }
  b -= K0_MOD;
  if (b < K0_LN) {
    const int tok = b * 16 + w * 4 + (lane >> 4), p = lane & 15;
    const u16* xp = x + (size_t)tok * D + 4 * p;
    u32x2 xv[9];
#pragma unroll
    for (int i = 0; i < 9; ++i) xv[i] = *(const u32x2*)(xp + 64 * i);
    float sm = 0.f;
#pragma unroll
    for (int i = 0; i < 9; ++i) sm += (bflo(xv[i][0]) + bfhi(xv[i][0])) + (bflo(xv[i][1]) + bfhi(xv[i][1]));
    sm += __shfl_xor(sm, 1); sm += __shfl_xor(sm, 2); sm += __shfl_xor(sm, 4); sm += __shfl_xor(sm, 8);
    const float mean = sm * (1.f / D);
    float q = 0.f;
#pragma unroll
    for (int i = 0; i < 9; ++i)
#pragma unroll
      for (int j = 0; j < 2; ++j) { float a0 = bflo(xv[i][j]) - mean, a1 = bfhi(xv[i][j]) - mean; q += a0 * a0; q += a1 * a1; }
    q += __shfl_xor(q, 1); q += __shfl_xor(q, 2); q += __shfl_xor(q, 4); q += __shfl_xor(q, 8);
    const float rstd = __builtin_amdgcn_rsqf(q * (1.f / D) + 1e-5f);
    char* dst = ws + NF_OFF + (size_t)(tok >> 6) * 73728 + ((tok >> 4) & 3) * 1024 + ((p >> 1) & 3) * 256 + (tok & 15) * 16 + (p & 1) * 8 + (p >> 3) * 4096;
#pragma unroll
    for (int i = 0; i < 9; ++i) {
      u32x2 o = {cvtpk((bflo(xv[i][0]) - mean) * rstd, (bfhi(xv[i][0]) - mean) * rstd),
                 cvtpk((bflo(xv[i][1]) - mean) * rstd, (bfhi(xv[i][1]) - mean) * rstd)};
      *(u32x2*)(dst + i * 8192) = o;
    }
    TS(0, 1); TSEND(0);
    return;
  }
  b -= K0_LN;
  {
    int u = b * 4 + w;
    int s = u % 18, tile = (u / 18) % 14, hq = u / (18 * 14);
    int g = lane >> 4, r = lane & 15;
    int col; bool ok = true;
    if (tile < 12) col = (tile >> 2) * 576 + (4 * hq + (tile & 3)) * 18 + r;
    else if (tile == 12) { int i = r >> 1; col = (i >> 2) * 576 + (4 * hq + (i & 3)) * 18 + 16 + (r & 1); }
    else { ok = r < 8; int i = (r >> 1) & 3; col = 1152 + (4 * hq + i) * 18 + 16 + (r & 1); }
    u16 v[8];
#pragma unroll
    for (int j = 0; j < 8; ++j) v[j] = wqkv[(size_t)(32 * s + 8 * g + j) * 1728 + col];
    u32x4 o;
#pragma unroll
    for (int j = 0; j < 4; ++j) o[j] = ok ? ((unsigned)v[2 * j] | ((unsigned)v[2 * j + 1] << 16)) : 0u;
    *(u32x4*)(ws + WQKVT_OFF + (size_t)u * 1024 + lane * 16) = o;
    TS(0, 1); TSEND(0);
  }
}

DI f32x4 mod4(const float* __restrict__ modp, int col) {
  const f32x4 a = *(const f32x4*)(modp + col), b = *(const f32x4*)(modp + 3456 + col), c = *(const f32x4*)(modp + 2 * 3456 + col),
              d = *(const f32x4*)(modp + 3 * 3456 + col);
  return f32x4{rbf(a[0] + b[0] + c[0] + d[0]), rbf(a[1] + b[1] + c[1] + d[1]), rbf(a[2] + b[2] + c[2] + d[2]), rbf(a[3] + b[3] + c[3] + d[3])};
}
DI f32x4 gain4(f32x4 v) { return f32x4{rbf(1.f + v[0]), rbf(1.f + v[1]), rbf(1.f + v[2]), rbf(1.f + v[3])}; }

constexpr float QSCALE = 0.23570226039551584f * 1.4426950408889634f;
DI u32x4 modulate8(u32x4 v, const LAS float* gp) {
  const f32x4 g0 = *(const LAS f32x4*)gp, g1 = *(const LAS f32x4*)(gp + 4), s0 = *(const LAS f32x4*)(gp + 576), s1 = *(const LAS f32x4*)(gp + 580);
  return u32x4{cvtpk(bflo(v[0]) * g0[0] + s0[0], bfhi(v[0]) * g0[1] + s0[1]), cvtpk(bflo(v[1]) * g0[2] + s0[2], bfhi(v[1]) * g0[3] + s0[3]),
               cvtpk(bflo(v[2]) * g1[0] + s1[0], bfhi(v[2]) * g1[1] + s1[1]), cvtpk(bflo(v[3]) * g1[2] + s1[2], bfhi(v[3]) * g1[3] + s1[3])};
}
__global__ void __launch_bounds__(512, 2) k1_qkv(char* __restrict__ ws) {
  __shared__ __attribute__((aligned(16))) char lds[73728 + 4608];
  const int tid = threadIdx.x, lane = tid & 63, w = __builtin_amdgcn_readfirstlane(tid >> 6);
  const int xcd = blockIdx.x & 7, idx = blockIdx.x >> 3;
  const int rb = (xcd >> 1) * 8 + (idx & 7), hq = (xcd & 1) * 4 + (idx >> 3);
  LAS char* hb = (LAS char*)lds;
  LAS float* modv = (LAS float*)(hb + 73728);
  const float* modp = (const float*)(ws + MODP_OFF);
  TSDECL; TS(1, 0);
  f32x4 mv;
  const bool domod = tid < 288;
  if (domod) mv = mod4(modp, tid < 144 ? D + 4 * tid : 4 * (tid - 144));
  const u32x4* np = (const u32x4*)(ws + NF_OFF + (size_t)rb * 73728) + lane;
  u32x4 xv[9];
#pragma unroll
  for (int i = 0; i < 8; ++i) xv[i] = np[(8 * w + i) * 64];
  xv[8] = np[(64 + w) * 64];
  constexpr int WIN = 16;
  u32x4 a[WIN];
  const int wt = w < 7 ? w : 6;
  const u32x4* ap = (const u32x4*)(ws + WQKVT_OFF + ((size_t)hq * 14 + 2 * wt) * 18 * 1024) + lane;
#pragma unroll
  for (int i = 0; i < WIN; ++i) a[i] = ap[((i & 1) * 18 + (i >> 1)) * 64];
  SBAR();
  if (domod) *(LAS f32x4*)(modv + 4 * tid) = tid < 144 ? gain4(mv) : mv;
  __syncthreads();
  TS(1, 1);
  {
    const int g8 = (lane >> 4) * 8;
#pragma unroll
    for (int i = 0; i < 8; ++i)
      *(LAS u32x4*)(hb + (8 * w + i) * 1024 + lane * 16) = modulate8(xv[i], modv + (2 * w + (i >> 2)) * 32 + g8);
    *(LAS u32x4*)(hb + (64 + w) * 1024 + lane * 16) = modulate8(xv[8], modv + (16 + (w >> 2)) * 32 + g8);
  }
  TS(1, 2);
  __syncthreads();
  TS(1, 3);
  const int g = lane >> 4, c = lane & 15;
  if (w == 7) {
    const int i = blockIdx.x * 64 + lane;
    if (i < 864) {
      const f32x4 v = mod4(modp, 4 * i);
      const int vec = i / 144;
      *(f32x4*)(ws + MODF_OFF + 16 * i) = (vec == 1 || vec == 4) ? gain4(v) : v;
    }
    TS(1, 4); TSEND(1);
    return;
  }
  f32x4 acc[2][4];
#pragma unroll
  for (int j = 0; j < 2; ++j)
#pragma unroll
    for (int t = 0; t < 4; ++t) acc[j][t] = f32x4{0.f, 0.f, 0.f, 0.f};
  const LAS char* b0 = hb + lane * 16;
  const bool n0 = (w == 4 || w == 5), n1 = (w >= 4);
#pragma unroll
  for (int s = 0; s < KS; ++s) {
    u32x4 bf[4];
#pragma unroll
    for (int t = 0; t < 4; ++t) bf[t] = lds128(b0 + (s * 4 + t) * 1024);
#pragma unroll
    for (int j = 0; j < 2; ++j) {
      const int i = 2 * s + j;
      const u32x4 cur = a[i % WIN];
      if (i + WIN < 36) a[i % WIN] = ap[(((i + WIN) & 1) * 18 + ((i + WIN) >> 1)) * 64];
      if (j == 0 ? n0 : n1) {
#pragma unroll
        for (int t = 0; t < 4; ++t) acc[j][t] = mfma16(bf[t], cur, acc[j][t]);
      } else {
#pragma unroll
        for (int t = 0; t < 4; ++t) acc[j][t] = mfma16(cur, bf[t], acc[j][t]);
      }
    }
    SBAR();
  }
  TS(1, 4);
#pragma unroll
  for (int j = 0; j < 2; ++j) {
    const int tile = 2 * w + j;
#pragma unroll
    for (int t = 0; t < 4; ++t) {
      const f32x4 v = acc[j][t];
      if (tile < 8) {
        const int tok = rb * 64 + t * 16 + c, h = 4 * hq + (tile & 3);
        const float sc = tile < 4 ? QSCALE : 1.f;
        u32x2 o = {cvtpk(v[0] * sc, v[1] * sc), cvtpk(v[2] * sc, v[3] * sc)};
        const int sub = ((g >> 1) * 32 + (tok & 31)) * 16 + (g & 1) * 8;
        char* dst = tile < 4 ? ws + QLO_OFF + ((size_t)h * 64 + (tok >> 5)) * 1024 + sub
                             : ws + KV_OFF + (size_t)h * KVH + (tok >> 5) * KVT + sub;
        *(u32x2*)dst = o;
      } else if (tile < 12 || tile == 13) {
        const int tok0 = rb * 64 + t * 16, kt = tok0 >> 5, sstep = (tok0 >> 4) & 1;
        int h, d; u32x2 o = {cvtpk(v[0], v[1]), cvtpk(v[2], v[3])};
        if (tile < 12) { h = 4 * hq + (tile & 3); d = c; }
        else if (c < 8) { h = 4 * hq + (c >> 1); d = 16 + (c & 1); }
        else { h = 4 * hq + (c & 3); d = c < 12 ? 18 : 19; const unsigned cv = c < 12 ? 0x3F803F80u : 0u; o = u32x2{cv, cv}; }
        *(u32x2*)(ws + KV_OFF + (size_t)h * KVH + kt * KVT + 1152 + sstep * 640 + (g & 1) * 320 + d * 16 + (g >> 1) * 8) = o;
      } else {
        const int tok = rb * 64 + t * 16 + c;
        const bool isq = g < 2;
        const float sc = isq ? QSCALE : 1.f;
#pragma unroll
        for (int e = 0; e < 2; ++e) {
          const int h = 4 * hq + 2 * (g & 1) + e;
          const unsigned o = cvtpk(v[2 * e] * sc, v[2 * e + 1] * sc);
          char* dst = isq ? ws + QHI_OFF + ((size_t)h * 2048 + tok) * 4
                          : ws + KV_OFF + (size_t)h * KVH + (tok >> 5) * KVT + 1024 + (tok & 31) * 4;
          *(unsigned*)dst = o;
        }
      }
    }
  }
  TS(1, 5); TSEND(1);
}

#define ATT_ORDER 0
#define ATT_VAR 0
DI float ex2(float x) {
  return __builtin_amdgcn_exp2f(x);
}
typedef short s16x4v __attribute__((ext_vector_type(4)));
DI f32x16 mfma32k8(u32x4 a, u32x4 b, f32x16 c) {
  return mfma32(a, b, c);
}
DI f32x16 mfmapv(u32x4 a, u32x4 b, f32x16 c) {
  return mfma32(a, b, c);
}
DI u32x4 ldsv(const LAS char* p, u32x4 old, bool act) {
  return *(const LAS u32x4*)p;
}
#define ATT_G(P, i0, S, j0) \
  e0 = ex2(S[j0]); e1 = ex2(S[j0 + 1]); e2 = ex2(S[j0 + 2]); e3 = ex2(S[j0 + 3]); \
  SBAR(); P[i0] = cvtpk(e0, e1); P[i0 + 1] = cvtpk(e2, e3); SBAR();
DI void attn_step(const LAS char* kp, const LAS char* hp, const LAS char* vp, int off, u32x4& ka, u32x4& kb, u32x4& v0, u32x4& v1,
                  const u32x4 q0, const u32x4 q1, const f32x16& cin, f32x16& sc, f32x16& sn, u32x4& p1, f32x16& o, bool vact) {
  u32x4 p0;
  float e0, e1, e2, e3;
  ATT_G(p0, 0, sc, 0)
  o = mfmapv(v1, p1, o);
  SBAR();
  v1 = ldsv(vp + off + 640, v1, vact);
  ATT_G(p0, 2, sc, 4)
  sn = mfma32k8(kb, q1, sn);
  SBAR();
  kb[0] = *(const LAS unsigned*)(hp + off + 2 * KVT);
  ATT_G(p1, 0, sc, 8)
  o = mfmapv(v0, p0, o);
  SBAR();
  v0 = ldsv(vp + off + KVT, v0, vact);
  ATT_G(p1, 2, sc, 12)
  sc = mfma32(ka, q0, cin);
  SBAR();
  ka = lds128(kp + off + 3 * KVT);
}
template <bool SUBMAX, bool STAGED>
DI void attn_all(const LAS char* kv, u32x4 q0, u32x4 q1, float negm, int lane, f32x16& o) {
  const LAS char* kp = kv + lane * 16;
  const LAS char* hp = kv + 1024 + (lane & 31) * 4;
  const LAS char* vp = kv + 1152 + (lane >> 5) * 320 + (lane & 31) * 16;
  f32x16 cin;
#pragma unroll
  for (int i = 0; i < 16; ++i) cin[i] = SUBMAX ? negm : 0.f;
  u32x4 ka = lds128(kp);
  u32x4 kb = {*(const LAS unsigned*)(hp), 0u, 0u, 0u};
  f32x16 sa = mfma32(ka, q0, cin), sb;
  sa = mfma32k8(kb, q1, sa);
  ka = lds128(kp + KVT);
  sb = mfma32(ka, q0, cin);
  ka = lds128(kp + 2 * KVT);
  kb[0] = *(const LAS unsigned*)(hp + KVT);
  const bool vact = (lane & 31) < 20;
  const u32x4 zz = {0u, 0u, 0u, 0u};
  u32x4 v0 = ldsv(vp, zz, vact), v1 = zz, p1 = zz;
#pragma unroll 1
  for (int c = 0; c < 8; ++c) {
    if (STAGED) {
      if (c == 1) asm volatile("s_waitcnt vmcnt(13)\n\ts_barrier" ::: "memory");
      else if (c == 2) asm volatile("s_waitcnt vmcnt(0)\n\ts_barrier" ::: "memory");
    }
#pragma unroll
    for (int u = 0; u < 8; u += 2) {
      attn_step(kp, hp, vp, u * KVT, ka, kb, v0, v1, q0, q1, cin, sa, sb, p1, o, vact);
      attn_step(kp, hp, vp, (u + 1) * KVT, ka, kb, v0, v1, q0, q1, cin, sb, sa, p1, o, vact);
    }
    kp += 8 * KVT; hp += 8 * KVT; vp += 8 * KVT;
  }
  o = mfmapv(v1, p1, o);
}
template <int NU, bool ISW1>
DI void retile_units(const u16* __restrict__ w1, const u16* __restrict__ w2, const u16* __restrict__ wo, char* __restrict__ ws,
                     LAS char* stage, int job, int lane) {
  asm volatile("s_barrier\n\ts_barrier\n\ts_barrier" ::: "memory");
  u32x4 raw[NU][4];
  const int rr = lane >> 3, cc = (lane & 7) * 8;
#pragma unroll
  for (int u = 0; u < NU; ++u) {
    if (ISW1) {
      const int cgp = job / 3, s = (job % 3) * 6 + u;
#pragma unroll
      for (int i = 0; i < 4; ++i) raw[u][i] = *(const u32x4*)(w1 + (size_t)(32 * s + 8 * i + rr) * F + cgp * 64 + cc);
    } else {
      const int un = min(2 * job + u, 1457);
      if (un < 1296) {
        const int sg = un % 144, n0 = (un / 144) * 64;
#pragma unroll
        for (int i = 0; i < 4; ++i) raw[u][i] = *(const u32x4*)(w2 + (size_t)(32 * sg + 8 * i + rr) * D + n0 + cc);
      } else {
        const int v = un - 1296, s = v % 18, n0 = (v / 18) * 64;
#pragma unroll
        for (int i = 0; i < 4; ++i) {
          const int r = (8 * i + rr) & 15, g = r >> 2, j = 4 * (i >> 1) + (r & 3), ci = 4 * s + g, G = ci / 9, r9 = ci % 9;
          const int k = r9 < 8 ? (4 * G + (r9 >> 1)) * 18 + 8 * (r9 & 1) + j : (4 * G + (j >> 1)) * 18 + 16 + (j & 1);
          raw[u][i] = *(const u32x4*)(wo + (size_t)k * D + n0 + cc);
        }
      }
    }
  }
  SBAR();
  const int g = lane >> 4, c = lane & 15;
  u32x4 bv[ISW1 ? NU : 1];
  if (ISW1) {
#pragma unroll
    for (int u = 0; u < NU; ++u) {
      const float* mp = (const float*)(ws + MODF_OFF) + ((c & 1) ? 3 : 4) * D + 32 * ((job % 3) * 6 + u) + 4 * g;
      const f32x4 lo = *(const f32x4*)mp, hi = *(const f32x4*)(mp + 16);
      bv[u] = c < 2 ? u32x4{cvtpk(lo[0], lo[1]), cvtpk(lo[2], lo[3]), cvtpk(hi[0], hi[1]), cvtpk(hi[2], hi[3])} : u32x4{0u, 0u, 0u, 0u};
    }
  }
  const rsrc_t rs = ws_rsrc(ws);
  f32x4 acc[4];
#pragma unroll
  for (int i = 0; i < 4; ++i) acc[i] = f32x4{0.f, 0.f, 0.f, 0.f};
  const LAS char* rp = stage + (4 * g + (c >> 2)) * 128 + (lane & 3) * 8;
#pragma unroll
  for (int u = 0; u < NU; ++u) {
    u32x2 lo[4], hi[4];
    *(LAS u32x4*)(stage + lane * 16) = raw[u][0];
    *(LAS u32x4*)(stage + 1024 + lane * 16) = raw[u][1];
#pragma unroll
    for (int i = 0; i < 4; ++i) lo[i] = tr_read(rp + i * 32);
    asm volatile("s_waitcnt lgkmcnt(0)" ::: "memory");
    *(LAS u32x4*)(stage + lane * 16) = raw[u][2];
    *(LAS u32x4*)(stage + 1024 + lane * 16) = raw[u][3];
#pragma unroll
    for (int i = 0; i < 4; ++i) hi[i] = tr_read(rp + i * 32);
    asm volatile("s_waitcnt lgkmcnt(0)" ::: "memory");
#pragma unroll
    for (int i = 0; i < 4; ++i) {
      const u32x4 fragv = {lo[i][0], lo[i][1], hi[i][0], hi[i][1]};
      size_t off;
      bool ok = true;
      if (ISW1) {
        const int Tn = (job / 3) * 4 + i, s = (job % 3) * 6 + u;
        off = W1T_OFF + ((size_t)(Tn / 36) * 648 + frag_index(Tn % 36, s)) * 1024;
        acc[i] = mfma16(fragv, bv[u], acc[i]);
      } else {
        const int un = 2 * job + u;
        ok = un < 1458;
        if (un < 1296) {
          const int sg = un % 144;
          off = W2T_OFF + ((size_t)(sg / 18) * 648 + frag_index((un / 144) * 4 + i, sg % 18)) * 1024;
        } else {
          const int v = min(un, 1457) - 1296;
          off = WOT_OFF + ((size_t)((v / 18) * 4 + i) * 18 + v % 18) * 1024;
        }
      }
      if (ok) bst16<AUX_RT>(rs, lane * 16, (unsigned)off, fragv);
    }
  }
  if (ISW1 && c < 2) {
#pragma unroll
    for (int i = 0; i < 4; ++i)
      *(f32x4*)(ws + CSB_OFF + ((size_t)((job % 3) * 2 + c) * F + ((job / 3) * 4 + i) * 16 + 4 * g) * 4) = acc[i];
  }
}
__global__ void __launch_bounds__(768, 3) k2_attn(const u16* __restrict__ w1, const u16* __restrict__ w2, const u16* __restrict__ wo,
                                                  char* __restrict__ ws) {
  __shared__ __attribute__((aligned(16))) char lds[163840];
  const int tid = threadIdx.x, lane = tid & 63, w = __builtin_amdgcn_readfirstlane(tid >> 6);
  if (w >= 8) {
    const int hid = blockIdx.x * 4 + (w - 8);
    LAS char* stage = (LAS char*)lds + KVH + (w - 8) * 2048;
    if (hid < 216) retile_units<6, true>(w1, w2, wo, ws, stage, hid, lane);
    else if (hid < 216 + 729) retile_units<2, false>(w1, w2, wo, ws, stage, hid - 216, lane);
    else asm volatile("s_barrier\n\ts_barrier\n\ts_barrier" ::: "memory");
    return;
  }
  const int xcd = blockIdx.x & 7, idx = blockIdx.x >> 3, h = 4 * xcd + (idx & 3), qb = idx >> 2;
  const int qt = qb * 8 + w;
  TSDECL; TS(2, 0);
  u32x4 q0 = *(const u32x4*)(ws + QLO_OFF + ((size_t)h * 64 + qt) * 1024 + lane * 16);
  unsigned qh = *(const unsigned*)(ws + QHI_OFF + ((size_t)h * 2048 + qt * 32 + (lane & 31)) * 4);
  const char* src = ws + KV_OFF + (size_t)h * KVH + w * 1024 + lane * 16;
  const unsigned lb = (unsigned)(size_t)(LAS char*)lds + w * 1024;
#pragma unroll
  for (int r = 0; r < 4; ++r) glds16(src + r * 8192, lb + r * 8192);
  asm volatile("" : "+v"(q0), "+v"(qh));
  WAIT_VM(0);
#pragma unroll
  for (int r = 4; r < 19; ++r) glds16(src + r * 8192, lb + r * 8192);
  const u32x4 q1 = {lane < 32 ? qh : 0u, 0u, 0u, 0u};
  const LAS char* kv = (const LAS char*)lds;
  f32x16 o;
#pragma unroll
  for (int i = 0; i < 16; ++i) o[i] = 0.f;
  asm volatile("s_barrier" ::: "memory");
  TS(2, 1);
  attn_all<false, true>(kv, q0, q1, 0.f, lane, o);
  TS(2, 5);
  float l = __shfl(o[10], lane & 31);
  bool bad = !(l >= 7.8886091e-31f && l <= 1.3292280e36f);
#pragma unroll
  for (int i = 0; i < 10; ++i) bad |= !(__builtin_fabsf(o[i]) <= 3.0e38f) && (i < 8 || lane < 32);
  if (__builtin_expect(__any(bad), 0) && ATT_VAR < 3) {
    float m = -3.0e38f;
    const LAS char* kp = kv + lane * 16;
    const LAS char* hp = kv + 1024 + (lane & 31) * 4;
    f32x16 z;
#pragma unroll
    for (int i = 0; i < 16; ++i) z[i] = 0.f;
#pragma unroll 1
    for (int t = 0; t < 64; ++t) {
      const u32x4 ka = lds128(kp + t * KVT);
      const u32x4 kb = {*(const LAS unsigned*)(hp + t * KVT), 0u, 0u, 0u};
      f32x16 sc = mfma32(ka, q0, z);
      sc = mfma32(kb, q1, sc);
#pragma unroll
      for (int i = 0; i < 16; ++i) m = __builtin_fmaxf(m, sc[i]);
    }
    m = __builtin_fmaxf(m, __shfl_xor(m, 32));
#pragma unroll
    for (int i = 0; i < 16; ++i) o[i] = 0.f;
    attn_all<true, false>(kv, q0, q1, -m, lane, o);
    l = __shfl(o[10], lane & 31);
  }
  const float rl = 1.f / l;
  const int q = qt * 32 + (lane & 31), hf = lane >> 5;
  char* ob = ws + OLO_OFF + ((size_t)h * 2048 + q) * 32 + hf * 8;
  *(u32x2*)(ob) = u32x2{cvtpk(o[0] * rl, o[1] * rl), cvtpk(o[2] * rl, o[3] * rl)};
  *(u32x2*)(ob + 16) = u32x2{cvtpk(o[4] * rl, o[5] * rl), cvtpk(o[6] * rl, o[7] * rl)};
  if (lane < 32) *(unsigned*)(ws + OHI_OFF + ((size_t)(h >> 2) * 2048 + q) * 16 + (h & 3) * 4) = cvtpk(o[8] * rl, o[9] * rl);
  TS(2, 6); TSEND(2);
}

__global__ void __launch_bounds__(576) k3_wo(const u16* __restrict__ x, char* __restrict__ ws) {
  __shared__ __attribute__((aligned(16))) char lds[36 * 1024 + 9 * 64 * 4];
  const int tid = threadIdx.x, lane = tid & 63, w = __builtin_amdgcn_readfirstlane(tid >> 6);
  const int rb = (blockIdx.x & 7) * 8 + ((blockIdx.x >> 3) & 7), cg = blockIdx.x >> 6;
  TSDECL; TS(3, 0);
  const int g = lane >> 4, c = lane & 15;
  const unsigned lb = (unsigned)(size_t)(LAS char*)lds;
#pragma unroll
  for (int i = 0; i < 4; ++i) {
    const int fi = w * 4 + i, s = fi >> 1, tt = fi & 1;
    const int ci = 4 * s + g, G = ci / 9, r9 = ci % 9, tok = rb * 32 + tt * 16 + c;
    const char* src = r9 < 8 ? ws + OLO_OFF + ((size_t)(4 * G + (r9 >> 1)) * 2048 + tok) * 32 + (r9 & 1) * 16
                             : ws + OHI_OFF + ((size_t)G * 2048 + tok) * 16;
    glds16(src, lb + fi * 1024);
  }
  const int T = cg * 9 + w;
  const u32x4* ap = (const u32x4*)(ws + WOT_OFF + (size_t)T * 18 * 1024) + lane;
  u32x4 a[18];
#pragma unroll
  for (int s = 0; s < 18; ++s) a[s] = ap[s * 64];
  u32x2 xr[2];
#pragma unroll
  for (int t = 0; t < 2; ++t) xr[t] = *(const u32x2*)(x + (size_t)(rb * 32 + t * 16 + c) * D + T * 16 + 4 * g);
  const f32x4 ga = *(const f32x4*)(ws + MODF_OFF + (2 * D + T * 16 + 4 * g) * 4);
  const f32x4 gb2 = *(const f32x4*)(ws + MODF_OFF + (4 * D + T * 16 + 4 * g) * 4);
  SBAR();
  WAIT_VM(0);
  __syncthreads();
  TS(3, 1);
  f32x4 acc[2] = {f32x4{0.f, 0.f, 0.f, 0.f}, f32x4{0.f, 0.f, 0.f, 0.f}};
  const LAS char* bp = (const LAS char*)lds + lane * 16;
#pragma unroll
  for (int s = 0; s < 18; ++s)
#pragma unroll
    for (int t = 0; t < 2; ++t) acc[t] = mfma16(a[s], lds128(bp + (s * 2 + t) * 1024), acc[t]);
  u32x4 xo;
  LAS float* red = (LAS float*)(lds + 36 * 1024);
#pragma unroll
  for (int t = 0; t < 2; ++t) {
    const unsigned o0 = cvtpk(bflo(xr[t][0]) + ga[0] * acc[t][0], bfhi(xr[t][0]) + ga[1] * acc[t][1]);
    const unsigned o1 = cvtpk(bflo(xr[t][1]) + ga[2] * acc[t][2], bfhi(xr[t][1]) + ga[3] * acc[t][3]);
    xo[2 * t] = o0; xo[2 * t + 1] = o1;
    const float v0 = bflo(o0), v1 = bfhi(o0), v2 = bflo(o1), v3 = bfhi(o1);
    const u32x2 og = {cvtpk(v0 * gb2[0], v1 * gb2[1]), cvtpk(v2 * gb2[2], v3 * gb2[3])};
    *(u32x2*)(ws + X1G_OFF + (size_t)(rb >> 1) * 73728 + ((T >> 1) * 4 + (rb & 1) * 2 + t) * 1024 + lane * 16 + (T & 1) * 8) = og;
    float s1 = (v0 + v1) + (v2 + v3), s2 = (v0 * v0 + v1 * v1) + (v2 * v2 + v3 * v3);
    s1 += __shfl_xor(s1, 16); s2 += __shfl_xor(s2, 16);
    s1 += __shfl_xor(s1, 32); s2 += __shfl_xor(s2, 32);
    if (g == 0) { red[w * 64 + t * 16 + c] = s1; red[w * 64 + 32 + t * 16 + c] = s2; }
  }
  *(u32x4*)(ws + X1_OFF + ((((size_t)(rb >> 1)) * 36 + T) * 2 + (rb & 1)) * 1024 + lane * 16) = xo;
  asm volatile("s_waitcnt lgkmcnt(0)\n\ts_barrier" ::: "memory");
  if (tid < 64) {
    float sm = 0.f;
#pragma unroll
    for (int i = 0; i < 9; ++i) sm += red[i * 64 + tid];
    ((float*)(ws + STAT_OFF))[(rb * 4 + cg) * 64 + tid] = sm;
  }
  TS(3, 2); TSEND(3);
}

#define K4_WINM 2
template <int NT>
DI void mlp_body(char* __restrict__ ws, LAS char* hb, LAS float* st, LAS float* csb, int w, int lane, int fr, int rb, f32x4 sv0, f32x4 sv1, f32x4 sv2 PROFARG) {
  constexpr int NF = NT * KS, WIN = K4_WINM * NT;
  const int T0 = NT == 5 ? 5 * w : 20 + 4 * (w - 4);
  const size_t wbase = (size_t)fr * 648 + (NT == 5 ? w * 90 : 360 + (w - 4) * 72);
  const rsrc_t rs = ws_rsrc(ws);
  const u32x4* __restrict__ a1 = (const u32x4*)(ws + W1T_OFF + wbase * 1024) + lane;
  const u32x4* __restrict__ a2 = (const u32x4*)(ws + W2T_OFF + wbase * 1024) + lane;
  u32x4 a[WIN];
#pragma unroll
  for (int i = 0; i < WIN; ++i) a[i] = a1[i * 64];
  SBAR();
  {
    const int tid = w * 64 + lane;
    if (tid < 64) {
      const float s1 = (sv0[0] + sv0[1]) + (sv0[2] + sv0[3]), s2 = (sv1[0] + sv1[1]) + (sv1[2] + sv1[3]);
      const float mean = s1 * (1.f / D);
      const float var = __builtin_fmaxf(s2 * (1.f / D) - mean * mean, 0.f);
      const float rstd = __builtin_amdgcn_rsqf(var + 1e-5f);
      st[2 * tid] = rstd; st[2 * tid + 1] = -mean * rstd;
    } else if (tid < 352) {
      *(LAS f32x4*)(csb + 4 * (tid - 64)) = sv0 + sv1 + sv2;
    }
  }
  asm volatile("s_waitcnt vmcnt(%0) lgkmcnt(0)\n\ts_barrier" :: "i"(WIN) : "memory");
  TS(4, 2);
  const LAS char* bp = hb + lane * 16;
  const int g = lane >> 4, c = lane & 15;
  f32x4 acc[NT][4];
#pragma unroll
  for (int j = 0; j < NT; ++j)
#pragma unroll
    for (int t = 0; t < 4; ++t) acc[j][t] = f32x4{0.f, 0.f, 0.f, 0.f};
#pragma unroll
  for (int s = 0; s < KS; ++s) {
    u32x4 bf[4];
#pragma unroll
    for (int t = 0; t < 4; ++t) bf[t] = lds128(bp + (s * 4 + t) * 1024);
#pragma unroll
    for (int j = 0; j < NT; ++j) {
      const int i = s * NT + j;
      const u32x4 cur = a[i % WIN];
      a[i % WIN] = i + WIN < NF ? a1[(i + WIN) * 64] : a2[(i + WIN - NF) * 64];
#pragma unroll
      for (int t = 0; t < 4; ++t) acc[j][t] = mfma16(cur, bf[t], acc[j][t]);
    }
    SBAR();
  }
  TS(4, 3);
  __syncthreads();
  TS(4, 4);
  {
    float rs[4], nm[4];
#pragma unroll
    for (int t = 0; t < 4; ++t) { rs[t] = st[2 * (t * 16 + c)]; nm[t] = st[2 * (t * 16 + c) + 1]; }
#pragma unroll
    for (int j = 0; j < NT; ++j) {
      const int T = T0 + j, s = T >> 1, half = T & 1;
      const f32x4 cs = *(const LAS f32x4*)(csb + T * 16 + 4 * g), bs = *(const LAS f32x4*)(csb + 576 + T * 16 + 4 * g);
#pragma unroll
      for (int t = 0; t < 4; ++t) {
        const f32x4 v = acc[j][t];
        float p[4];
#pragma unroll
        for (int r = 0; r < 4; ++r) p[r] = silu_f(v[r] * rs[t] + (cs[r] * nm[t] + bs[r]));
        *(LAS u32x2*)(hb + (s * 4 + t) * 1024 + lane * 16 + half * 8) = u32x2{cvtpk(p[0], p[1]), cvtpk(p[2], p[3])};
      }
    }
  }
  TS(4, 5);
  __syncthreads();
  TS(4, 6);
#pragma unroll
  for (int j = 0; j < NT; ++j)
#pragma unroll
    for (int t = 0; t < 4; ++t) acc[j][t] = f32x4{0.f, 0.f, 0.f, 0.f};
#pragma unroll
  for (int s = 0; s < KS; ++s) {
    u32x4 bf[4];
#pragma unroll
    for (int t = 0; t < 4; ++t) bf[t] = lds128(bp + (s * 4 + t) * 1024);
#pragma unroll
    for (int j = 0; j < NT; ++j) {
      const int i = s * NT + j;
      const u32x4 cur = a[(NF + i) % WIN];
      if (i + WIN < NF) a[(NF + i) % WIN] = a2[(i + WIN) * 64];
#pragma unroll
      for (int t = 0; t < 4; ++t) acc[j][t] = mfma16(cur, bf[t], acc[j][t]);
    }
    SBAR();
  }
  TS(4, 7);
#pragma unroll
  for (int j = 0; j < NT; ++j) {
    const int T = T0 + j;
#pragma unroll
    for (int tp = 0; tp < 2; ++tp) {
      const f32x4 v0 = acc[j][2 * tp], v1 = acc[j][2 * tp + 1];
      u32x4 o = {cvtpk(v0[0], v0[1]), cvtpk(v0[2], v0[3]), cvtpk(v1[0], v1[1]), cvtpk(v1[2], v1[3])};
      bst16<AUX_PART>(rs, lane * 16, (unsigned)(PART_OFF + ((((size_t)fr * 32 + rb) * 36 + T) * 2 + tp) * 1024), o);
    }
  }
  TS(4, 8); TSEND(4);
}
#define K4_MAP 1
__global__ void __launch_bounds__(512, 2) k4_mlp(char* __restrict__ ws) {
  __shared__ __attribute__((aligned(16))) char lds[73728 + 512 + 4608];
  const int tid = threadIdx.x, lane = tid & 63, w = __builtin_amdgcn_readfirstlane(tid >> 6);
  const int xcd = blockIdx.x & 7, idx = blockIdx.x >> 3;
  const int rb = (xcd >> 2) * 16 + (idx & 15), fr = (xcd & 3) * 2 + (idx >> 4);
  LAS char* hb = (LAS char*)lds;
  LAS float* st = (LAS float*)(hb + 73728);
  LAS float* csb = st + 128;
  TSDECL; TS(4, 0);
  f32x4 sv0 = {0.f, 0.f, 0.f, 0.f}, sv1 = sv0, sv2 = sv0;
  if (w == 0) {
    const float* sp = (const float*)(ws + STAT_OFF) + (size_t)(rb * 2 + (tid >> 5)) * 256 + (tid & 31);
    sv0 = f32x4{sp[0], sp[64], sp[128], sp[192]};
    sv1 = f32x4{sp[32], sp[96], sp[160], sp[224]};
  } else {
    const int i = tid < 352 ? tid - 64 : 0;
    const char* cp = ws + CSB_OFF + ((size_t)(i >= 144) * F + fr * 576 + 4 * (i >= 144 ? i - 144 : i)) * 4;
    sv0 = *(const f32x4*)cp; sv1 = *(const f32x4*)(cp + 2 * F * 4); sv2 = *(const f32x4*)(cp + 4 * F * 4);
  }
  const char* src = ws + X1G_OFF + (size_t)rb * 73728 + w * 9216 + lane * 16;
  const unsigned lb = (unsigned)(size_t)hb + w * 9216;
#pragma unroll
  for (int i = 0; i < 9; ++i) glds16(src + i * 1024, lb + i * 1024);
  if (w < 4) mlp_body<5>(ws, hb, st, csb, w, lane, fr, rb, sv0, sv1, sv2 PROFPASS);
  else mlp_body<4>(ws, hb, st, csb, w, lane, fr, rb, sv0, sv1, sv2 PROFPASS);
}

__global__ void __launch_bounds__(576) k5_out(char* __restrict__ ws, u16* __restrict__ out) {
  const int tid = threadIdx.x, lane = tid & 63, w = tid >> 6;
  TSDECL; TS(5, 0);
  const int rb = blockIdx.x >> 3, sub = blockIdx.x & 7;
  const int unit = sub * 9 + w, T = unit >> 1, tp = unit & 1;
  const int g = lane >> 4, c = lane & 15;
  u32x4 p[8];
#pragma unroll
  for (int f = 0; f < 8; ++f) p[f] = *(const u32x4*)(ws + PART_OFF + ((((size_t)f * 32 + rb) * 36 + T) * 2 + tp) * 1024 + lane * 16);
  const int col = T * 16 + 4 * g;
  const u32x4 xr = *(const u32x4*)(ws + X1_OFF + (((size_t)rb * 36 + T) * 2 + tp) * 1024 + lane * 16);
  const f32x4 gb = *(const f32x4*)(ws + MODF_OFF + (5 * D + col) * 4);
#pragma unroll
  for (int t = 0; t < 2; ++t) {
    float a[4] = {0.f, 0.f, 0.f, 0.f};
#pragma unroll
    for (int f = 0; f < 8; ++f) {
      a[0] += bflo(p[f][2 * t]); a[1] += bfhi(p[f][2 * t]);
      a[2] += bflo(p[f][2 * t + 1]); a[3] += bfhi(p[f][2 * t + 1]);
    }
    u32x2 o = {cvtpk(bflo(xr[2 * t]) + gb[0] * a[0], bfhi(xr[2 * t]) + gb[1] * a[1]),
               cvtpk(bflo(xr[2 * t + 1]) + gb[2] * a[2], bfhi(xr[2 * t + 1]) + gb[3] * a[3])};
    *(u32x2*)(out + (size_t)(rb * 64 + tp * 32 + t * 16 + c) * D + col) = o;
  }
  TS(5, 1); TSEND(5);
}

extern "C" void kernel_launch(void* const* d_in, const int* in_sizes, int n_in, void* d_out, int out_size, void* d_ws,
                              size_t ws_size, hipStream_t stream) {
  const u16* x = (const u16*)d_in[0];
  const u16* wqkv = (const u16*)d_in[1];
  const u16* wo = (const u16*)d_in[2];
  const u16* w1 = (const u16*)d_in[4];
  const u16* w2 = (const u16*)d_in[5];
  const u16* c = (const u16*)d_in[6];
  const u16* wmod = (const u16*)d_in[7];
  char* ws = (char*)d_ws;
  k0_prep<<<K0_MOD + K0_LN + K0_QKV_BLKS, 256, 0, stream>>>(c, wmod, wqkv, x, ws);
  k1_qkv<<<256, 512, 0, stream>>>(ws);
  k2_attn<<<256, 768, 0, stream>>>(w1, w2, wo, ws);
  k3_wo<<<256, 576, 0, stream>>>(x, ws);
  k4_mlp<<<256, 512, 0, stream>>>(ws);
  k5_out<<<256, 576, 0, stream>>>(ws, (u16*)d_out);
}
```

```cpp
#include <hip/hip_runtime.h>
#include <hip/hip_bf16.h>
#include <stdint.h>

typedef unsigned short u16;
typedef short bf16x8 __attribute__((ext_vector_type(8)));
typedef short s16x4 __attribute__((ext_vector_type(4)));
typedef float f32x4 __attribute__((ext_vector_type(4)));
typedef float f32x16 __attribute__((ext_vector_type(16)));
typedef unsigned u32x4 __attribute__((ext_vector_type(4)));
typedef unsigned u32x2 __attribute__((ext_vector_type(2)));
#define LAS __attribute__((address_space(3)))
#define GAS __attribute__((address_space(1)))
#define DI __device__ __forceinline__

constexpr int S = 2048, D = 576, NH = 32, DH = 18, F = 4608;
constexpr int KS = 18;

constexpr size_t MODP_OFF = 0;
constexpr size_t WQKVT_OFF = 0x10000;
constexpr size_t WOT_OFF = WQKVT_OFF + 8 * 14 * 18 * 1024;
constexpr size_t W1T_OFF = WOT_OFF + 36 * 18 * 1024;
constexpr size_t W2T_OFF = W1T_OFF + 8 * 648 * 1024;
constexpr size_t QLO_OFF = W2T_OFF + 8 * 648 * 1024;
constexpr size_t QHI_OFF = QLO_OFF + 32 * 64 * 1024;
constexpr int KVT = 2432;
constexpr int KVH = 64 * KVT;
constexpr size_t KV_OFF = QHI_OFF + 32 * 2048 * 4;
constexpr size_t OLO_OFF = KV_OFF + (size_t)32 * KVH;
constexpr size_t OHI_OFF = OLO_OFF + 32 * 2048 * 32;
constexpr size_t X1_OFF = OHI_OFF + 8 * 2048 * 16;
constexpr size_t X1G_OFF = X1_OFF + (size_t)S * D * 2;
constexpr size_t NF_OFF = X1G_OFF + (size_t)S * D * 2;
constexpr size_t STAT_OFF = NF_OFF + (size_t)S * D * 2;
constexpr size_t CSB_OFF = STAT_OFF + 64 * 4 * 64 * 4;
constexpr size_t MODF_OFF = CSB_OFF + 3 * 2 * 4608 * 4;
constexpr size_t PART_OFF = CSB_OFF + 0x20000;
constexpr size_t WS_END = PART_OFF + (size_t)8 * S * D * 2;

#define TSDECL
#define TS(kid, i)
#define TSEND(kid)
#define PROFARG
#define PROFPASS
template <int I> struct IC { static constexpr int v = I; };
template <int B, int E, class Fn> __device__ __forceinline__ void sfor(Fn&& f) {
  if constexpr (B < E) { f(IC<B>{}); sfor<B + 1, E>(f); }
}
#define LAM(x) [&](auto x) __attribute__((always_inline))
DI float bf2f(unsigned v) { return __builtin_bit_cast(float, v << 16); }
DI float bflo(unsigned v) { return __builtin_bit_cast(float, v << 16); }
DI float bfhi(unsigned v) { return __builtin_bit_cast(float, v & 0xffff0000u); }
typedef __bf16 bf16x2_t __attribute__((ext_vector_type(2)));
typedef float f32x2 __attribute__((ext_vector_type(2)));
DI unsigned cvtpk(float lo, float hi) {
  return __builtin_bit_cast(unsigned, __builtin_convertvector(f32x2{lo, hi}, bf16x2_t));
}
DI float rbf(float x) { return bflo(cvtpk(x, x)); }
DI f32x4 mfma16(u32x4 a, u32x4 b, f32x4 c) {
  return __builtin_amdgcn_mfma_f32_16x16x32_bf16(__builtin_bit_cast(bf16x8, a), __builtin_bit_cast(bf16x8, b), c, 0, 0, 0);
}
DI f32x16 mfma32(u32x4 a, u32x4 b, f32x16 c) {
  return __builtin_amdgcn_mfma_f32_32x32x16_bf16(__builtin_bit_cast(bf16x8, a), __builtin_bit_cast(bf16x8, b), c, 0, 0, 0);
}
DI void glds16(const void* g, unsigned lds_base) {
  unsigned sv;
  asm volatile("s_mov_b32 %0, m0\n\ts_mov_b32 m0, %2\n\ts_nop 0\n\tglobal_load_lds_dwordx4 %1, off\n\ts_mov_b32 m0, %0"
               : "=&s"(sv) : "v"(g), "s"(lds_base) : "memory");
}
typedef __amdgpu_buffer_rsrc_t rsrc_t;
DI rsrc_t ws_rsrc(const char* ws) { return __builtin_amdgcn_make_buffer_rsrc((void*)ws, (short)0, (int)WS_END, 0x00020000); }
template <int AUX> DI void bst16(rsrc_t r, unsigned voff, unsigned soff, u32x4 v) { __builtin_amdgcn_raw_buffer_store_b128(v, r, voff, soff, AUX); }
template <int AUX> DI void bst8(rsrc_t r, unsigned voff, unsigned soff, u32x2 v) { __builtin_amdgcn_raw_buffer_store_b64(v, r, voff, soff, AUX); }
template <int AUX> DI void bst4(rsrc_t r, unsigned voff, unsigned soff, unsigned v) { __builtin_amdgcn_raw_buffer_store_b32(v, r, voff, soff, AUX); }
DI u32x4 bld16(rsrc_t r, unsigned voff, unsigned soff) { return __builtin_amdgcn_raw_buffer_load_b128(r, voff, soff, 0); }
#define AUX_RT 16
#define AUX_PART 16
#define WAIT_VM(n) asm volatile("s_waitcnt vmcnt(" #n ")" ::: "memory")
#define SBAR() __builtin_amdgcn_sched_barrier(0)
DI float silu_f(float x) { return x * __builtin_amdgcn_rcpf(1.f + __builtin_amdgcn_exp2f(-1.4426950408889634f * x)); }
typedef short v4i16_t __attribute__((ext_vector_type(4)));
DI u32x2 tr_read(const LAS char* p) {
  return __builtin_bit_cast(u32x2, __builtin_amdgcn_ds_read_tr16_b64_v4i16((LAS v4i16_t*)p));
}
DI u32x4 lds128(const LAS char* p) { return *(const LAS u32x4*)p; }

DI int frag_index(int T, int s) {
  return T < 20 ? (T / 5) * 90 + s * 5 + (T % 5) : 360 + ((T - 20) >> 2) * 72 + s * 4 + ((T - 20) & 3);
}

DI int frag_index1(int T, int s) {
  if (T < 16) { const int w = T >> 1; return (w < 4 ? 90 * w : 360 + 72 * (w - 4)) + 2 * s + (T & 1); }
  if (T < 28) { const int w = (T - 16) / 3; return 90 * w + 36 + 3 * s + (T - 16) % 3; }
  const int w = 4 + ((T - 28) >> 1);
  return 360 + 72 * (w - 4) + 36 + 2 * s + (T & 1);
}

constexpr int K0_MOD = 216, K0_LN = 128, K0_QKV_BLKS = 2016 / 4;
__global__ void __launch_bounds__(256) k0_prep(const u16* __restrict__ c, const u16* __restrict__ wmod, const u16* __restrict__ wqkv,
                                               const u16* __restrict__ x, char* __restrict__ ws) {
  __shared__ __attribute__((aligned(16))) char lds[1024];
  const int tid = threadIdx.x, lane = tid & 63, w = __builtin_amdgcn_readfirstlane(tid >> 6);
  int b = blockIdx.x;
  TSDECL; TS(0, 0);
  if (b < K0_MOD) {
    const int cc = b % 54, ks = b / 54;
    const int ch = tid & 7, rg = tid >> 3;
    float acc[8];
#pragma unroll
    for (int j = 0; j < 8; ++j) acc[j] = 0.f;
    u32x4 wv[5];
    float sv[5];
#pragma unroll
    for (int i = 0; i < 5; ++i) {
      int r = rg + 32 * i;
      bool ok = r < 144;
      int k = ks * 144 + (ok ? r : 0);
      wv[i] = *(const u32x4*)(wmod + (size_t)k * 3456 + cc * 64 + ch * 8);
      float cv = bf2f(c[k]);
      sv[i] = ok ? rbf(cv / (1.f + __expf(-cv))) : 0.f;
    }
#pragma unroll
    for (int i = 0; i < 5; ++i)
#pragma unroll
      for (int j = 0; j < 4; ++j) {
        acc[2 * j] += sv[i] * bflo(wv[i][j]);
        acc[2 * j + 1] += sv[i] * bfhi(wv[i][j]);
      }
#pragma unroll
    for (int j = 0; j < 8; ++j) {
      acc[j] += __shfl_xor(acc[j], 8);
      acc[j] += __shfl_xor(acc[j], 16);
      acc[j] += __shfl_xor(acc[j], 32);
    }
    float* red = (float*)lds;
    if (lane < 8)
#pragma unroll
      for (int j = 0; j < 8; ++j) red[w * 64 + lane * 8 + j] = acc[j];
    __syncthreads();
    if (tid < 64) ((float*)(ws + MODP_OFF))[ks * 3456 + cc * 64 + tid] = red[tid] + red[64 + tid] + red[128 + tid] + red[192 + tid];
    TS(0, 1); TSEND(0);
    return;
  }
  b -= K0_MOD;
  if (b < K0_LN) {
    const int tok = b * 16 + w * 4 + (lane >> 4), p = lane & 15;
    const u16* xp = x + (size_t)tok * D + 4 * p;
    u32x2 xv[9];
#pragma unroll
    for (int i = 0; i < 9; ++i) xv[i] = *(const u32x2*)(xp + 64 * i);
    float sm = 0.f;
#pragma unroll
    for (int i = 0; i < 9; ++i) sm += (bflo(xv[i][0]) + bfhi(xv[i][0])) + (bflo(xv[i][1]) + bfhi(xv[i][1]));
    sm += __shfl_xor(sm, 1); sm += __shfl_xor(sm, 2); sm += __shfl_xor(sm, 4); sm += __shfl_xor(sm, 8);
    const float mean = sm * (1.f / D);
    float q = 0.f;
#pragma unroll
    for (int i = 0; i < 9; ++i)
#pragma unroll
      for (int j = 0; j < 2; ++j) { float a0 = bflo(xv[i][j]) - mean, a1 = bfhi(xv[i][j]) - mean; q += a0 * a0; q += a1 * a1; }
    q += __shfl_xor(q, 1); q += __shfl_xor(q, 2); q += __shfl_xor(q, 4); q += __shfl_xor(q, 8);
    const float rstd = __builtin_amdgcn_rsqf(q * (1.f / D) + 1e-5f);
    char* dst = ws + NF_OFF + (size_t)(tok >> 6) * 73728 + ((tok >> 4) & 3) * 1024 + ((p >> 1) & 3) * 256 + (tok & 15) * 16 + (p & 1) * 8 + (p >> 3) * 4096;
#pragma unroll
    for (int i = 0; i < 9; ++i) {
      u32x2 o = {cvtpk((bflo(xv[i][0]) - mean) * rstd, (bfhi(xv[i][0]) - mean) * rstd),
                 cvtpk((bflo(xv[i][1]) - mean) * rstd, (bfhi(xv[i][1]) - mean) * rstd)};
      *(u32x2*)(dst + i * 8192) = o;
    }
    TS(0, 1); TSEND(0);
    return;
  }
  b -= K0_LN;
  {
    int u = b * 4 + w;
    int s = u % 18, tile = (u / 18) % 14, hq = u / (18 * 14);
    int g = lane >> 4, r = lane & 15;
    int col; bool ok = true;
    if (tile < 12) col = (tile >> 2) * 576 + (4 * hq + (tile & 3)) * 18 + r;
    else if (tile == 12) { int i = r >> 1; col = (i >> 2) * 576 + (4 * hq + (i & 3)) * 18 + 16 + (r & 1); }
    else { ok = r < 8; int i = (r >> 1) & 3; col = 1152 + (4 * hq + i) * 18 + 16 + (r & 1); }
    u16 v[8];
#pragma unroll
    for (int j = 0; j < 8; ++j) v[j] = wqkv[(size_t)(32 * s + 8 * g + j) * 1728 + col];
    u32x4 o;
#pragma unroll
    for (int j = 0; j < 4; ++j) o[j] = ok ? ((unsigned)v[2 * j] | ((unsigned)v[2 * j + 1] << 16)) : 0u;
    *(u32x4*)(ws + WQKVT_OFF + (size_t)u * 1024 + lane * 16) = o;
    TS(0, 1); TSEND(0);
  }
}

DI f32x4 mod4(const float* __restrict__ modp, int col) {
  const f32x4 a = *(const f32x4*)(modp + col), b = *(const f32x4*)(modp + 3456 + col), c = *(const f32x4*)(modp + 2 * 3456 + col),
              d = *(const f32x4*)(modp + 3 * 3456 + col);
  return f32x4{rbf(a[0] + b[0] + c[0] + d[0]), rbf(a[1] + b[1] + c[1] + d[1]), rbf(a[2] + b[2] + c[2] + d[2]), rbf(a[3] + b[3] + c[3] + d[3])};
}
DI f32x4 gain4(f32x4 v) { return f32x4{rbf(1.f + v[0]), rbf(1.f + v[1]), rbf(1.f + v[2]), rbf(1.f + v[3])}; }

constexpr float QSCALE = 0.23570226039551584f * 1.4426950408889634f;
DI u32x4 modulate8(u32x4 v, const LAS float* gp) {
  const f32x4 g0 = *(const LAS f32x4*)gp, g1 = *(const LAS f32x4*)(gp + 4), s0 = *(const LAS f32x4*)(gp + 576), s1 = *(const LAS f32x4*)(gp + 580);
  return u32x4{cvtpk(bflo(v[0]) * g0[0] + s0[0], bfhi(v[0]) * g0[1] + s0[1]), cvtpk(bflo(v[1]) * g0[2] + s0[2], bfhi(v[1]) * g0[3] + s0[3]),
               cvtpk(bflo(v[2]) * g1[0] + s1[0], bfhi(v[2]) * g1[1] + s1[1]), cvtpk(bflo(v[3]) * g1[2] + s1[2], bfhi(v[3]) * g1[3] + s1[3])};
}
__global__ void __launch_bounds__(512, 2) k1_qkv(char* __restrict__ ws) {
  __shared__ __attribute__((aligned(16))) char lds[73728 + 4608];
  const int tid = threadIdx.x, lane = tid & 63, w = __builtin_amdgcn_readfirstlane(tid >> 6);
  const int xcd = blockIdx.x & 7, idx = blockIdx.x >> 3;
  const int rb = (xcd >> 1) * 8 + (idx & 7), hq = (xcd & 1) * 4 + (idx >> 3);
  LAS char* hb = (LAS char*)lds;
  LAS float* modv = (LAS float*)(hb + 73728);
  const float* modp = (const float*)(ws + MODP_OFF);
  TSDECL; TS(1, 0);
  f32x4 mv;
  const bool domod = tid < 288;
  if (domod) mv = mod4(modp, tid < 144 ? D + 4 * tid : 4 * (tid - 144));
  const u32x4* np = (const u32x4*)(ws + NF_OFF + (size_t)rb * 73728) + lane;
  u32x4 xv[9];
#pragma unroll
  for (int i = 0; i < 8; ++i) xv[i] = np[(8 * w + i) * 64];
  xv[8] = np[(64 + w) * 64];
  constexpr int WIN = 16;
  u32x4 a[WIN];
  const int wt = w < 7 ? w : 6;
  const u32x4* ap = (const u32x4*)(ws + WQKVT_OFF + ((size_t)hq * 14 + 2 * wt) * 18 * 1024) + lane;
#pragma unroll
  for (int i = 0; i < WIN; ++i) a[i] = ap[((i & 1) * 18 + (i >> 1)) * 64];
  SBAR();
  if (domod) *(LAS f32x4*)(modv + 4 * tid) = tid < 144 ? gain4(mv) : mv;
  __syncthreads();
  TS(1, 1);
  {
    const int g8 = (lane >> 4) * 8;
#pragma unroll
    for (int i = 0; i < 8; ++i)
      *(LAS u32x4*)(hb + (8 * w + i) * 1024 + lane * 16) = modulate8(xv[i], modv + (2 * w + (i >> 2)) * 32 + g8);
    *(LAS u32x4*)(hb + (64 + w) * 1024 + lane * 16) = modulate8(xv[8], modv + (16 + (w >> 2)) * 32 + g8);
  }
  TS(1, 2);
  __syncthreads();
  TS(1, 3);
  const int g = lane >> 4, c = lane & 15;
  if (w == 7) {
    const int i = blockIdx.x * 64 + lane;
    if (i < 864) {
      const f32x4 v = mod4(modp, 4 * i);
      const int vec = i / 144;
      *(f32x4*)(ws + MODF_OFF + 16 * i) = (vec == 1 || vec == 4) ? gain4(v) : v;
    }
    TS(1, 4); TSEND(1);
    return;
  }
  f32x4 acc[2][4];
#pragma unroll
  for (int j = 0; j < 2; ++j)
#pragma unroll
    for (int t = 0; t < 4; ++t) acc[j][t] = f32x4{0.f, 0.f, 0.f, 0.f};
  const LAS char* b0 = hb + lane * 16;
  const bool n0 = (w == 4 || w == 5), n1 = (w >= 4);
#pragma unroll
  for (int s = 0; s < KS; ++s) {
    u32x4 bf[4];
#pragma unroll
    for (int t = 0; t < 4; ++t) bf[t] = lds128(b0 + (s * 4 + t) * 1024);
#pragma unroll
    for (int j = 0; j < 2; ++j) {
      const int i = 2 * s + j;
      const u32x4 cur = a[i % WIN];
      if (i + WIN < 36) a[i % WIN] = ap[(((i + WIN) & 1) * 18 + ((i + WIN) >> 1)) * 64];
      if (j == 0 ? n0 : n1) {
#pragma unroll
        for (int t = 0; t < 4; ++t) acc[j][t] = mfma16(bf[t], cur, acc[j][t]);
      } else {
#pragma unroll
        for (int t = 0; t < 4; ++t) acc[j][t] = mfma16(cur, bf[t], acc[j][t]);
      }
    }
    SBAR();
  }
  TS(1, 4);
#pragma unroll
  for (int j = 0; j < 2; ++j) {
    const int tile = 2 * w + j;
#pragma unroll
    for (int t = 0; t < 4; ++t) {
      const f32x4 v = acc[j][t];
      if (tile < 8) {
        const int tok = rb * 64 + t * 16 + c, h = 4 * hq + (tile & 3);
        const float sc = tile < 4 ? QSCALE : 1.f;
        u32x2 o = {cvtpk(v[0] * sc, v[1] * sc), cvtpk(v[2] * sc, v[3] * sc)};
        const int sub = ((g >> 1) * 32 + (tok & 31)) * 16 + (g & 1) * 8;
        char* dst = tile < 4 ? ws + QLO_OFF + ((size_t)h * 64 + (tok >> 5)) * 1024 + sub
                             : ws + KV_OFF + (size_t)h * KVH + (tok >> 5) * KVT + sub;
        *(u32x2*)dst = o;
      } else if (tile < 12 || tile == 13) {
        const int tok0 = rb * 64 + t * 16, kt = tok0 >> 5, sstep = (tok0 >> 4) & 1;
        int h, d; u32x2 o = {cvtpk(v[0], v[1]), cvtpk(v[2], v[3])};
        if (tile < 12) { h = 4 * hq + (tile & 3); d = c; }
        else if (c < 8) { h = 4 * hq + (c >> 1); d = 16 + (c & 1); }
        else { h = 4 * hq + (c & 3); d = c < 12 ? 18 : 19; const unsigned cv = c < 12 ? 0x3F803F80u : 0u; o = u32x2{cv, cv}; }
        *(u32x2*)(ws + KV_OFF + (size_t)h * KVH + kt * KVT + 1152 + sstep * 640 + (g & 1) * 320 + d * 16 + (g >> 1) * 8) = o;
      } else {
        const int tok = rb * 64 + t * 16 + c;
        const bool isq = g < 2;
        const float sc = isq ? QSCALE : 1.f;
#pragma unroll
        for (int e = 0; e < 2; ++e) {
          const int h = 4 * hq + 2 * (g & 1) + e;
          const unsigned o = cvtpk(v[2 * e] * sc, v[2 * e + 1] * sc);
          char* dst = isq ? ws + QHI_OFF + ((size_t)h * 2048 + tok) * 4
                          : ws + KV_OFF + (size_t)h * KVH + (tok >> 5) * KVT + 1024 + (tok & 31) * 4;
          *(unsigned*)dst = o;
        }
      }
    }
  }
  TS(1, 5); TSEND(1);
}

#define ATT_ORDER 0
#define ATT_VAR 0
DI float ex2(float x) {
  return __builtin_amdgcn_exp2f(x);
}
typedef short s16x4v __attribute__((ext_vector_type(4)));
DI f32x16 mfma32k8(u32x4 a, u32x4 b, f32x16 c) {
  return mfma32(a, b, c);
}
DI f32x16 mfmapv(u32x4 a, u32x4 b, f32x16 c) {
  return mfma32(a, b, c);
}
DI u32x4 ldsv(const LAS char* p, u32x4 old, bool act) {
  return *(const LAS u32x4*)p;
}
#define ATT_G(P, i0, S, j0) \
  e0 = ex2(S[j0]); e1 = ex2(S[j0 + 1]); e2 = ex2(S[j0 + 2]); e3 = ex2(S[j0 + 3]); \
  SBAR(); P[i0] = cvtpk(e0, e1); P[i0 + 1] = cvtpk(e2, e3); SBAR();
DI void attn_step(const LAS char* kp, const LAS char* hp, const LAS char* vp, int off, u32x4& ka, u32x4& kb, u32x4& v0, u32x4& v1,
                  const u32x4 q0, const u32x4 q1, const f32x16& cin, f32x16& sc, f32x16& sn, u32x4& p1, f32x16& o, bool vact) {
  u32x4 p0;
  float e0, e1, e2, e3;
  ATT_G(p0, 0, sc, 0)
  o = mfmapv(v1, p1, o);
  SBAR();
  v1 = ldsv(vp + off + 640, v1, vact);
  ATT_G(p0, 2, sc, 4)
  sn = mfma32k8(kb, q1, sn);
  SBAR();
  kb[0] = *(const LAS unsigned*)(hp + off + 2 * KVT);
  ATT_G(p1, 0, sc, 8)
  o = mfmapv(v0, p0, o);
  SBAR();
  v0 = ldsv(vp + off + KVT, v0, vact);
  ATT_G(p1, 2, sc, 12)
  sc = mfma32(ka, q0, cin);
  SBAR();
  ka = lds128(kp + off + 3 * KVT);
}
template <bool SUBMAX, bool STAGED>
DI void attn_all(const LAS char* kv, u32x4 q0, u32x4 q1, float negm, int lane, f32x16& o) {
  const LAS char* kp = kv + lane * 16;
  const LAS char* hp = kv + 1024 + (lane & 31) * 4;
  const LAS char* vp = kv + 1152 + (lane >> 5) * 320 + (lane & 31) * 16;
  f32x16 cin;
#pragma unroll
  for (int i = 0; i < 16; ++i) cin[i] = SUBMAX ? negm : 0.f;
  u32x4 ka = lds128(kp);
  u32x4 kb = {*(const LAS unsigned*)(hp), 0u, 0u, 0u};
  f32x16 sa = mfma32(ka, q0, cin), sb;
  sa = mfma32k8(kb, q1, sa);
  ka = lds128(kp + KVT);
  sb = mfma32(ka, q0, cin);
  ka = lds128(kp + 2 * KVT);
  kb[0] = *(const LAS unsigned*)(hp + KVT);
  const bool vact = (lane & 31) < 20;
  const u32x4 zz = {0u, 0u, 0u, 0u};
  u32x4 v0 = ldsv(vp, zz, vact), v1 = zz, p1 = zz;
#pragma unroll 1
  for (int c = 0; c < 8; ++c) {
    if (STAGED) {
      if (c == 1) asm volatile("s_waitcnt vmcnt(13)\n\ts_barrier" ::: "memory");
      else if (c == 2) asm volatile("s_waitcnt vmcnt(0)\n\ts_barrier" ::: "memory");
    }
#pragma unroll
    for (int u = 0; u < 8; u += 2) {
      attn_step(kp, hp, vp, u * KVT, ka, kb, v0, v1, q0, q1, cin, sa, sb, p1, o, vact);
      attn_step(kp, hp, vp, (u + 1) * KVT, ka, kb, v0, v1, q0, q1, cin, sb, sa, p1, o, vact);
    }
    kp += 8 * KVT; hp += 8 * KVT; vp += 8 * KVT;
  }
  o = mfmapv(v1, p1, o);
}
template <int NU, bool ISW1>
DI void retile_units(const u16* __restrict__ w1, const u16* __restrict__ w2, const u16* __restrict__ wo, char* __restrict__ ws,
                     LAS char* stage, int job, int lane) {
  asm volatile("s_barrier\n\ts_barrier\n\ts_barrier" ::: "memory");
  u32x4 raw[NU][4];
  const int rr = lane >> 3, cc = (lane & 7) * 8;
#pragma unroll
  for (int u = 0; u < NU; ++u) {
    if (ISW1) {
      const int cgp = job / 3, s = (job % 3) * 6 + u;
#pragma unroll
      for (int i = 0; i < 4; ++i) raw[u][i] = *(const u32x4*)(w1 + (size_t)(32 * s + 8 * i + rr) * F + cgp * 64 + cc);
    } else {
      const int un = min(2 * job + u, 1457);
      if (un < 1296) {
        const int sg = un % 144, n0 = (un / 144) * 64;
#pragma unroll
        for (int i = 0; i < 4; ++i) raw[u][i] = *(const u32x4*)(w2 + (size_t)(32 * sg + 8 * i + rr) * D + n0 + cc);
      } else {
        const int v = un - 1296, s = v % 18, n0 = (v / 18) * 64;
#pragma unroll
        for (int i = 0; i < 4; ++i) {
          const int r = (8 * i + rr) & 15, g = r >> 2, j = 4 * (i >> 1) + (r & 3), ci = 4 * s + g, G = ci / 9, r9 = ci % 9;
          const int k = r9 < 8 ? (4 * G + (r9 >> 1)) * 18 + 8 * (r9 & 1) + j : (4 * G + (j >> 1)) * 18 + 16 + (j & 1);
          raw[u][i] = *(const u32x4*)(wo + (size_t)k * D + n0 + cc);
        }
      }
    }
  }
  SBAR();
  const int g = lane >> 4, c = lane & 15;
  u32x4 bv[ISW1 ? NU : 1];
  if (ISW1) {
#pragma unroll
    for (int u = 0; u < NU; ++u) {
      const float* mp = (const float*)(ws + MODF_OFF) + ((c & 1) ? 3 : 4) * D + 32 * ((job % 3) * 6 + u) + 4 * g;
      const f32x4 lo = *(const f32x4*)mp, hi = *(const f32x4*)(mp + 16);
      bv[u] = c < 2 ? u32x4{cvtpk(lo[0], lo[1]), cvtpk(lo[2], lo[3]), cvtpk(hi[0], hi[1]), cvtpk(hi[2], hi[3])} : u32x4{0u, 0u, 0u, 0u};
    }
  }
  const rsrc_t rs = ws_rsrc(ws);
  f32x4 acc[4];
#pragma unroll
  for (int i = 0; i < 4; ++i) acc[i] = f32x4{0.f, 0.f, 0.f, 0.f};
  const LAS char* rp = stage + (4 * g + (c >> 2)) * 128 + (lane & 3) * 8;
#pragma unroll
  for (int u = 0; u < NU; ++u) {
    u32x2 lo[4], hi[4];
    *(LAS u32x4*)(stage + lane * 16) = raw[u][0];
    *(LAS u32x4*)(stage + 1024 + lane * 16) = raw[u][1];
#pragma unroll
    for (int i = 0; i < 4; ++i) lo[i] = tr_read(rp + i * 32);
    asm volatile("s_waitcnt lgkmcnt(0)" ::: "memory");
    *(LAS u32x4*)(stage + lane * 16) = raw[u][2];
    *(LAS u32x4*)(stage + 1024 + lane * 16) = raw[u][3];
#pragma unroll
    for (int i = 0; i < 4; ++i) hi[i] = tr_read(rp + i * 32);
    asm volatile("s_waitcnt lgkmcnt(0)" ::: "memory");
#pragma unroll
    for (int i = 0; i < 4; ++i) {
      const u32x4 fragv = {lo[i][0], lo[i][1], hi[i][0], hi[i][1]};
      size_t off;
      bool ok = true;
      if (ISW1) {
        const int Tn = (job / 3) * 4 + i, s = (job % 3) * 6 + u;
        off = W1T_OFF + ((size_t)(Tn / 36) * 648 + frag_index1(Tn % 36, s)) * 1024;
        acc[i] = mfma16(fragv, bv[u], acc[i]);
      } else {
        const int un = 2 * job + u;
        ok = un < 1458;
        if (un < 1296) {
          const int sg = un % 144;
          off = W2T_OFF + ((size_t)(sg / 18) * 648 + frag_index((un / 144) * 4 + i, sg % 18)) * 1024;
        } else {
          const int v = min(un, 1457) - 1296;
          off = WOT_OFF + ((size_t)((v / 18) * 4 + i) * 18 + v % 18) * 1024;
        }
      }
      if (ok) bst16<AUX_RT>(rs, lane * 16, (unsigned)off, fragv);
    }
  }
  if (ISW1 && c < 2) {
#pragma unroll
    for (int i = 0; i < 4; ++i)
      *(f32x4*)(ws + CSB_OFF + ((size_t)((job % 3) * 2 + c) * F + ((job / 3) * 4 + i) * 16 + 4 * g) * 4) = acc[i];
  }
}
__global__ void __launch_bounds__(768, 3) k2_attn(const u16* __restrict__ w1, const u16* __restrict__ w2, const u16* __restrict__ wo,
                                                  char* __restrict__ ws) {
  __shared__ __attribute__((aligned(16))) char lds[163840];
  const int tid = threadIdx.x, lane = tid & 63, w = __builtin_amdgcn_readfirstlane(tid >> 6);
  if (w >= 8) {
    const int hid = blockIdx.x * 4 + (w - 8);
    LAS char* stage = (LAS char*)lds + KVH + (w - 8) * 2048;
    if (hid < 216) retile_units<6, true>(w1, w2, wo, ws, stage, hid, lane);
    else if (hid < 216 + 729) retile_units<2, false>(w1, w2, wo, ws, stage, hid - 216, lane);
    else asm volatile("s_barrier\n\ts_barrier\n\ts_barrier" ::: "memory");
    return;
  }
  const int xcd = blockIdx.x & 7, idx = blockIdx.x >> 3, h = 4 * xcd + (idx & 3), qb = idx >> 2;
  const int qt = qb * 8 + w;
  TSDECL; TS(2, 0);
  u32x4 q0 = *(const u32x4*)(ws + QLO_OFF + ((size_t)h * 64 + qt) * 1024 + lane * 16);
  unsigned qh = *(const unsigned*)(ws + QHI_OFF + ((size_t)h * 2048 + qt * 32 + (lane & 31)) * 4);
  const char* src = ws + KV_OFF + (size_t)h * KVH + w * 1024 + lane * 16;
  const unsigned lb = (unsigned)(size_t)(LAS char*)lds + w * 1024;
#pragma unroll
  for (int r = 0; r < 4; ++r) glds16(src + r * 8192, lb + r * 8192);
  asm volatile("" : "+v"(q0), "+v"(qh));
  WAIT_VM(0);
#pragma unroll
  for (int r = 4; r < 19; ++r) glds16(src + r * 8192, lb + r * 8192);
  const u32x4 q1 = {lane < 32 ? qh : 0u, 0u, 0u, 0u};
  const LAS char* kv = (const LAS char*)lds;
  f32x16 o;
#pragma unroll
  for (int i = 0; i < 16; ++i) o[i] = 0.f;
  asm volatile("s_barrier" ::: "memory");
  TS(2, 1);
  attn_all<false, true>(kv, q0, q1, 0.f, lane, o);
  TS(2, 5);
  float l = __shfl(o[10], lane & 31);
  bool bad = !(l >= 7.8886091e-31f && l <= 1.3292280e36f);
#pragma unroll
  for (int i = 0; i < 10; ++i) bad |= !(__builtin_fabsf(o[i]) <= 3.0e38f) && (i < 8 || lane < 32);
  if (__builtin_expect(__any(bad), 0) && ATT_VAR < 3) {
    float m = -3.0e38f;
    const LAS char* kp = kv + lane * 16;
    const LAS char* hp = kv + 1024 + (lane & 31) * 4;
    f32x16 z;
#pragma unroll
    for (int i = 0; i < 16; ++i) z[i] = 0.f;
#pragma unroll 1
    for (int t = 0; t < 64; ++t) {
      const u32x4 ka = lds128(kp + t * KVT);
      const u32x4 kb = {*(const LAS unsigned*)(hp + t * KVT), 0u, 0u, 0u};
      f32x16 sc = mfma32(ka, q0, z);
      sc = mfma32(kb, q1, sc);
#pragma unroll
      for (int i = 0; i < 16; ++i) m = __builtin_fmaxf(m, sc[i]);
    }
    m = __builtin_fmaxf(m, __shfl_xor(m, 32));
#pragma unroll
    for (int i = 0; i < 16; ++i) o[i] = 0.f;
    attn_all<true, false>(kv, q0, q1, -m, lane, o);
    l = __shfl(o[10], lane & 31);
  }
  const float rl = 1.f / l;
  const int q = qt * 32 + (lane & 31), hf = lane >> 5;
  char* ob = ws + OLO_OFF + ((size_t)h * 2048 + q) * 32 + hf * 8;
  *(u32x2*)(ob) = u32x2{cvtpk(o[0] * rl, o[1] * rl), cvtpk(o[2] * rl, o[3] * rl)};
  *(u32x2*)(ob + 16) = u32x2{cvtpk(o[4] * rl, o[5] * rl), cvtpk(o[6] * rl, o[7] * rl)};
  if (lane < 32) *(unsigned*)(ws + OHI_OFF + ((size_t)(h >> 2) * 2048 + q) * 16 + (h & 3) * 4) = cvtpk(o[8] * rl, o[9] * rl);
  TS(2, 6); TSEND(2);
}

__global__ void __launch_bounds__(576) k3_wo(const u16* __restrict__ x, char* __restrict__ ws) {
  __shared__ __attribute__((aligned(16))) char lds[36 * 1024 + 9 * 64 * 4];
  const int tid = threadIdx.x, lane = tid & 63, w = __builtin_amdgcn_readfirstlane(tid >> 6);
  const int rb = (blockIdx.x & 7) * 8 + ((blockIdx.x >> 3) & 7), cg = blockIdx.x >> 6;
  TSDECL; TS(3, 0);
  const int g = lane >> 4, c = lane & 15;
  const unsigned lb = (unsigned)(size_t)(LAS char*)lds;
#pragma unroll
  for (int i = 0; i < 4; ++i) {
    const int fi = w * 4 + i, s = fi >> 1, tt = fi & 1;
    const int ci = 4 * s + g, G = ci / 9, r9 = ci % 9, tok = rb * 32 + tt * 16 + c;
    const char* src = r9 < 8 ? ws + OLO_OFF + ((size_t)(4 * G + (r9 >> 1)) * 2048 + tok) * 32 + (r9 & 1) * 16
                             : ws + OHI_OFF + ((size_t)G * 2048 + tok) * 16;
    glds16(src, lb + fi * 1024);
  }
  const int T = cg * 9 + w;
  const u32x4* ap = (const u32x4*)(ws + WOT_OFF + (size_t)T * 18 * 1024) + lane;
  u32x4 a[18];
#pragma unroll
  for (int s = 0; s < 18; ++s) a[s] = ap[s * 64];
  u32x2 xr[2];
#pragma unroll
  for (int t = 0; t < 2; ++t) xr[t] = *(const u32x2*)(x + (size_t)(rb * 32 + t * 16 + c) * D + T * 16 + 4 * g);
  const f32x4 ga = *(const f32x4*)(ws + MODF_OFF + (2 * D + T * 16 + 4 * g) * 4);
  const f32x4 gb2 = *(const f32x4*)(ws + MODF_OFF + (4 * D + T * 16 + 4 * g) * 4);
  SBAR();
  WAIT_VM(0);
  __syncthreads();
  TS(3, 1);
  f32x4 acc[2] = {f32x4{0.f, 0.f, 0.f, 0.f}, f32x4{0.f, 0.f, 0.f, 0.f}};
  const LAS char* bp = (const LAS char*)lds + lane * 16;
#pragma unroll
  for (int s = 0; s < 18; ++s)
#pragma unroll
    for (int t = 0; t < 2; ++t) acc[t] = mfma16(a[s], lds128(bp + (s * 2 + t) * 1024), acc[t]);
  u32x4 xo;
  LAS float* red = (LAS float*)(lds + 36 * 1024);
#pragma unroll
  for (int t = 0; t < 2; ++t) {
    const unsigned o0 = cvtpk(bflo(xr[t][0]) + ga[0] * acc[t][0], bfhi(xr[t][0]) + ga[1] * acc[t][1]);
    const unsigned o1 = cvtpk(bflo(xr[t][1]) + ga[2] * acc[t][2], bfhi(xr[t][1]) + ga[3] * acc[t][3]);
    xo[2 * t] = o0; xo[2 * t + 1] = o1;
    const float v0 = bflo(o0), v1 = bfhi(o0), v2 = bflo(o1), v3 = bfhi(o1);
    const u32x2 og = {cvtpk(v0 * gb2[0], v1 * gb2[1]), cvtpk(v2 * gb2[2], v3 * gb2[3])};
    *(u32x2*)(ws + X1G_OFF + (size_t)(rb >> 1) * 73728 + ((T >> 1) * 4 + (rb & 1) * 2 + t) * 1024 + lane * 16 + (T & 1) * 8) = og;
    float s1 = (v0 + v1) + (v2 + v3), s2 = (v0 * v0 + v1 * v1) + (v2 * v2 + v3 * v3);
    s1 += __shfl_xor(s1, 16); s2 += __shfl_xor(s2, 16);
    s1 += __shfl_xor(s1, 32); s2 += __shfl_xor(s2, 32);
    if (g == 0) { red[w * 64 + t * 16 + c] = s1; red[w * 64 + 32 + t * 16 + c] = s2; }
  }
  *(u32x4*)(ws + X1_OFF + ((((size_t)(rb >> 1)) * 36 + T) * 2 + (rb & 1)) * 1024 + lane * 16) = xo;
  asm volatile("s_waitcnt lgkmcnt(0)\n\ts_barrier" ::: "memory");
  if (tid < 64) {
    float sm = 0.f;
#pragma unroll
    for (int i = 0; i < 9; ++i) sm += red[i * 64 + tid];
    ((float*)(ws + STAT_OFF))[(rb * 4 + cg) * 64 + tid] = sm;
  }
  TS(3, 2); TSEND(3);
}

#define K4_WIN 8
struct Silu { float z[4], e[4]; unsigned p[2]; };
constexpr int SOPS = 27;
#define PIN(x) asm volatile("" : "+v"(x))
template <int k>
DI void silu_op(Silu& q, const f32x4& v, const f32x4& cs, const f32x4& bs, float rs, float nm, LAS char* dst) {
  if constexpr (k < 4) { q.z[k] = __builtin_fmaf(cs[k], nm, bs[k]); PIN(q.z[k]); }
  else if constexpr (k < 8) { q.z[k - 4] = __builtin_fmaf(v[k - 4], rs, q.z[k - 4]); PIN(q.z[k - 4]); }
  else if constexpr (k < 12) { q.e[k - 8] = __builtin_amdgcn_exp2f(q.z[k - 8]); PIN(q.e[k - 8]); }
  else if constexpr (k < 16) { q.e[k - 12] = 1.f + q.e[k - 12]; PIN(q.e[k - 12]); }
  else if constexpr (k < 20) { q.e[k - 16] = __builtin_amdgcn_rcpf(q.e[k - 16]); PIN(q.e[k - 16]); }
  else if constexpr (k < 24) { q.e[k - 20] = q.z[k - 20] * q.e[k - 20]; PIN(q.e[k - 20]); }
  else if constexpr (k < 26) { q.p[k - 24] = cvtpk(q.e[2 * (k - 24)], q.e[2 * (k - 24) + 1]); PIN(q.p[k - 24]); }
  else *(LAS u32x2*)dst = u32x2{q.p[0], q.p[1]};
}
template <int NT>
DI void mlp_body(char* __restrict__ ws, LAS char* hb, LAS char* h1, LAS float* st, LAS float* csb, int w, int lane, int fr, int rb,
                 f32x4 sv0, f32x4 sv1, f32x4 sv2 PROFARG) {
  constexpr int NB = NT - 2, NF = NT * KS, WIN = K4_WIN;
  const int T0 = NT == 5 ? 5 * w : 20 + 4 * (w - 4);
  const int TA = 2 * w, TB = NT == 5 ? 16 + 3 * w : 28 + 2 * (w - 4);
  const unsigned wbase = (unsigned)(fr * 648 + (NT == 5 ? w * 90 : 360 + (w - 4) * 72)) * 1024u;
  const rsrc_t rs_ = ws_rsrc(ws);
  const unsigned vo = lane * 16;
  const unsigned o1 = (unsigned)W1T_OFF + wbase, o2 = (unsigned)W2T_OFF + wbase;
#define K4_EXP 0
#define WLOAD(i) bld16(rs_, vo, (i) < NF ? o1 + (i) * 1024u : o2 + ((i) - NF) * 1024u)
  u32x4 a[WIN];
#pragma unroll
  for (int i = 0; i < WIN; ++i) a[i] = WLOAD(i);
  SBAR();
  constexpr float NL2E = -1.4426950408889634f;
  {
    const int tid = w * 64 + lane;
    if (tid < 64) {
      const float s1 = (sv0[0] + sv0[1]) + (sv0[2] + sv0[3]), s2 = (sv1[0] + sv1[1]) + (sv1[2] + sv1[3]);
      const float mean = s1 * (1.f / D);
      const float var = __builtin_fmaxf(s2 * (1.f / D) - mean * mean, 0.f);
      const float rstd = __builtin_amdgcn_rsqf(var + 1e-5f);
      st[2 * tid] = rstd * NL2E; st[2 * tid + 1] = -mean * rstd;
    } else if (tid < 352) {
      *(LAS f32x4*)(csb + 4 * (tid - 64)) = (sv0 + sv1 + sv2) * NL2E;
    }
  }
  asm volatile("s_waitcnt vmcnt(%0) lgkmcnt(0)\n\ts_barrier" :: "i"(WIN) : "memory");
  TS(4, 2);
  unsigned bp0 = (unsigned)(size_t)hb + lane * 16, bp1 = bp0 + 40960, hp0 = (unsigned)(size_t)h1 + lane * 16, hp1 = hp0 + 40960;
  asm volatile("" : "+v"(bp0), "+v"(bp1), "+v"(hp0), "+v"(hp1));
#define XFRAG(f) lds128((const LAS char*)(size_t)((f) < 40 ? bp0 + (f) * 1024 : bp1 + ((f) - 40) * 1024))
#define HFRAG(f) lds128((const LAS char*)(size_t)((f) < 40 ? hp0 + (f) * 1024 : hp1 + ((f) - 40) * 1024))
  const int g = lane >> 4, c = lane & 15;
  f32x4 accA[2][4], accB[NB][4], acc[NT][4];
  u32x4 bf[2][4];
  const f32x4 z4 = {0.f, 0.f, 0.f, 0.f};
#pragma unroll
  for (int t = 0; t < 4; ++t) bf[0][t] = XFRAG(t);
#pragma unroll
  for (int s = 0; s < KS; ++s) {
#pragma unroll
    for (int t = 0; t < 4; ++t) bf[(s + 1) & 1][t] = XFRAG(((s + 1) % KS) * 4 + t);
    SBAR();
#pragma unroll
    for (int j = 0; j < 2; ++j) {
      const int i = s * 2 + j;
#pragma unroll
      for (int t = 0; t < 4; ++t) accA[j][t] = mfma16(a[i % WIN], bf[s & 1][t], s == 0 ? z4 : accA[j][t]);
      SBAR();
      a[i % WIN] = WLOAD(i + WIN);
      SBAR();
    }
  }
  TS(4, 3);
  float rs[4], nm[4];
#pragma unroll
  for (int t = 0; t < 4; ++t) { rs[t] = st[2 * (t * 16 + c)]; nm[t] = st[2 * (t * 16 + c) + 1]; }
  const LAS float* csg = csb + 4 * g;
  f32x4 cs[2], bs[2];
  cs[0] = *(const LAS f32x4*)(csg + TA * 16); bs[0] = *(const LAS f32x4*)(csg + 576 + TA * 16);
  cs[1] = *(const LAS f32x4*)(csg + TA * 16 + 16); bs[1] = *(const LAS f32x4*)(csg + 576 + TA * 16 + 16);
  Silu sq;
  sfor<0, KS>(LAM(s_) {
    constexpr int s = s_.v, M = 16 * NB * 4, TOT = 8 * SOPS;
    if constexpr (s + 1 < KS) {
#pragma unroll
      for (int t = 0; t < 4; ++t) bf[(s + 1) & 1][t] = XFRAG((s + 1) * 4 + t);
    }
    SBAR();
    sfor<0, NB>(LAM(j_) {
      constexpr int j = j_.v, i = 36 + s * NB + j;
      sfor<0, 4>(LAM(t_) {
        constexpr int t = t_.v, m = (s * NB + j) * 4 + t;
        accB[j][t] = mfma16(a[i % WIN], bf[s & 1][t], s == 0 ? z4 : accB[j][t]);
        PIN(accB[j][t]);
        if constexpr (m < M) {
          sfor<m * TOT / M, (m + 1) * TOT / M>(LAM(o_) {
            constexpr int o = o_.v, q = o / SOPS, jj = q >> 2, tt = q & 3;
            silu_op<o % SOPS>(sq, accA[jj][tt], cs[jj], bs[jj], rs[tt], nm[tt], h1 + (w * 4 + tt) * 1024 + lane * 16 + jj * 8);
          });
        }
        SBAR();
      });
      a[i % WIN] = WLOAD(i + WIN);
      SBAR();
    });
  });
  TS(4, 4);
  cs[0] = *(const LAS f32x4*)(csg + TB * 16); bs[0] = *(const LAS f32x4*)(csg + 576 + TB * 16);
  cs[1] = *(const LAS f32x4*)(csg + TB * 16 + 16); bs[1] = *(const LAS f32x4*)(csg + 576 + TB * 16 + 16);
  asm volatile("s_waitcnt lgkmcnt(0)\n\ts_barrier" ::: "memory");
  TS(4, 5);
#pragma unroll
  for (int t = 0; t < 4; ++t) bf[0][t] = HFRAG(t);
  sfor<0, 8>(LAM(s_) {
    constexpr int s = s_.v, M = 8 * NT * 4, TOT = NB * 4 * SOPS;
    if constexpr (s + 1 < 8) {
#pragma unroll
      for (int t = 0; t < 4; ++t) bf[(s + 1) & 1][t] = HFRAG((s + 1) * 4 + t);
    }
    SBAR();
    sfor<0, NT>(LAM(j_) {
      constexpr int j = j_.v, i = NF + s * NT + j;
      sfor<0, 4>(LAM(t_) {
        constexpr int t = t_.v, m = (s * NT + j) * 4 + t;
        acc[j][t] = mfma16(a[i % WIN], bf[s & 1][t], s == 0 ? z4 : acc[j][t]);
        PIN(acc[j][t]);
        sfor<m * TOT / M, (m + 1) * TOT / M>(LAM(o_) {
          constexpr int o = o_.v, q = o / SOPS, jj = q >> 2, tt = q & 3;
          const int T = TB + jj;
          if constexpr (NB == 3 && o == 4 * SOPS + 8) {
            cs[0] = *(const LAS f32x4*)(csg + TB * 16 + 32); bs[0] = *(const LAS f32x4*)(csg + 576 + TB * 16 + 32);
          }
          silu_op<o % SOPS>(sq, accB[jj][tt], cs[jj & 1], bs[jj & 1], rs[tt], nm[tt], h1 + ((T >> 1) * 4 + tt) * 1024 + lane * 16 + (T & 1) * 8);
        });
        SBAR();
      });
      a[i % WIN] = WLOAD(i + WIN);
      SBAR();
    });
  });
  TS(4, 6);
  asm volatile("s_waitcnt lgkmcnt(0)\n\ts_barrier" ::: "memory");
  TS(4, 7);
#pragma unroll
  for (int t = 0; t < 4; ++t) bf[0][t] = HFRAG(32 + t);
#pragma unroll
  for (int s = 8; s < KS; ++s) {
    if (s + 1 < KS) {
#pragma unroll
      for (int t = 0; t < 4; ++t) bf[(s + 1) & 1][t] = HFRAG((s + 1) * 4 + t);
    }
    SBAR();
#pragma unroll
    for (int j = 0; j < NT; ++j) {
      const int i = NF + s * NT + j;
#pragma unroll
      for (int t = 0; t < 4; ++t) acc[j][t] = mfma16(a[i % WIN], bf[s & 1][t], acc[j][t]);
      SBAR();
      if (i + WIN < 2 * NF) a[i % WIN] = WLOAD(i + WIN);
      SBAR();
    }
  }
  TS(4, 8);
#pragma unroll
  for (int j = 0; j < NT; ++j) {
    const int T = T0 + j;
#pragma unroll
    for (int tp = 0; tp < 2; ++tp) {
      const f32x4 v0 = acc[j][2 * tp], v1 = acc[j][2 * tp + 1];
      u32x4 o = {cvtpk(v0[0], v0[1]), cvtpk(v0[2], v0[3]), cvtpk(v1[0], v1[1]), cvtpk(v1[2], v1[3])};
      bst16<AUX_PART>(rs_, vo, (unsigned)(PART_OFF + ((((size_t)fr * 32 + rb) * 36 + T) * 2 + tp) * 1024), o);
    }
  }
  TS(4, 9); TSEND(4);
#undef WLOAD
#undef XFRAG
#undef HFRAG
}
#define K4_MAP 2
__global__ void __launch_bounds__(512, 2) k4_mlp(char* __restrict__ ws) {
  __shared__ __attribute__((aligned(16))) char lds[2 * 73728 + 512 + 4608];
  const int tid = threadIdx.x, lane = tid & 63, w = __builtin_amdgcn_readfirstlane(tid >> 6);
  const int xcd = blockIdx.x & 7, idx = blockIdx.x >> 3;
  const int rb = (xcd >> 1) * 8 + (idx & 7), fr = (xcd & 1) * 4 + (idx >> 3);
  LAS char* hb = (LAS char*)lds;
  LAS char* h1 = hb + 73728;
  LAS float* st = (LAS float*)(hb + 2 * 73728);
  LAS float* csb = st + 128;
  TSDECL; TS(4, 0);
  f32x4 sv0 = {0.f, 0.f, 0.f, 0.f}, sv1 = sv0, sv2 = sv0;
  if (w == 0) {
    const float* sp = (const float*)(ws + STAT_OFF) + (size_t)(rb * 2 + (tid >> 5)) * 256 + (tid & 31);
    sv0 = f32x4{sp[0], sp[64], sp[128], sp[192]};
    sv1 = f32x4{sp[32], sp[96], sp[160], sp[224]};
  } else {
    const int i = tid < 352 ? tid - 64 : 0;
    const char* cp = ws + CSB_OFF + ((size_t)(i >= 144) * F + fr * 576 + 4 * (i >= 144 ? i - 144 : i)) * 4;
    sv0 = *(const f32x4*)cp; sv1 = *(const f32x4*)(cp + 2 * F * 4); sv2 = *(const f32x4*)(cp + 4 * F * 4);
  }
  const char* src = ws + X1G_OFF + (size_t)rb * 73728 + w * 9216 + lane * 16;
  const unsigned lb = (unsigned)(size_t)hb + w * 9216;
#pragma unroll
  for (int i = 0; i < 9; ++i) glds16(src + i * 1024, lb + i * 1024);
  if (w < 4) mlp_body<5>(ws, hb, h1, st, csb, w, lane, fr, rb, sv0, sv1, sv2 PROFPASS);
  else mlp_body<4>(ws, hb, h1, st, csb, w, lane, fr, rb, sv0, sv1, sv2 PROFPASS);
}

__global__ void __launch_bounds__(576) k5_out(char* __restrict__ ws, u16* __restrict__ out) {
  const int tid = threadIdx.x, lane = tid & 63, w = tid >> 6;
  TSDECL; TS(5, 0);
  const int rb = blockIdx.x >> 3, sub = blockIdx.x & 7;
  const int unit = sub * 9 + w, T = unit >> 1, tp = unit & 1;
  const int g = lane >> 4, c = lane & 15;
  u32x4 p[8];
#pragma unroll
  for (int f = 0; f < 8; ++f) p[f] = *(const u32x4*)(ws + PART_OFF + ((((size_t)f * 32 + rb) * 36 + T) * 2 + tp) * 1024 + lane * 16);
  const int col = T * 16 + 4 * g;
  const u32x4 xr = *(const u32x4*)(ws + X1_OFF + (((size_t)rb * 36 + T) * 2 + tp) * 1024 + lane * 16);
  const f32x4 gb = *(const f32x4*)(ws + MODF_OFF + (5 * D + col) * 4) * -0.6931471805599453f;
#pragma unroll
  for (int t = 0; t < 2; ++t) {
    float a[4] = {0.f, 0.f, 0.f, 0.f};
#pragma unroll
    for (int f = 0; f < 8; ++f) {
      a[0] += bflo(p[f][2 * t]); a[1] += bfhi(p[f][2 * t]);
      a[2] += bflo(p[f][2 * t + 1]); a[3] += bfhi(p[f][2 * t + 1]);
    }
    u32x2 o = {cvtpk(bflo(xr[2 * t]) + gb[0] * a[0], bfhi(xr[2 * t]) + gb[1] * a[1]),
               cvtpk(bflo(xr[2 * t + 1]) + gb[2] * a[2], bfhi(xr[2 * t + 1]) + gb[3] * a[3])};
    *(u32x2*)(out + (size_t)(rb * 64 + tp * 32 + t * 16 + c) * D + col) = o;
  }
  TS(5, 1); TSEND(5);
}

extern "C" void kernel_launch(void* const* d_in, const int* in_sizes, int n_in, void* d_out, int out_size, void* d_ws,
                              size_t ws_size, hipStream_t stream) {
  const u16* x = (const u16*)d_in[0];
  const u16* wqkv = (const u16*)d_in[1];
  const u16* wo = (const u16*)d_in[2];
  const u16* w1 = (const u16*)d_in[4];
  const u16* w2 = (const u16*)d_in[5];
  const u16* c = (const u16*)d_in[6];
  const u16* wmod = (const u16*)d_in[7];
  char* ws = (char*)d_ws;
  k0_prep<<<K0_MOD + K0_LN + K0_QKV_BLKS, 256, 0, stream>>>(c, wmod, wqkv, x, ws);
  k1_qkv<<<256, 512, 0, stream>>>(ws);
  k2_attn<<<256, 768, 0, stream>>>(w1, w2, wo, ws);
  k3_wo<<<256, 576, 0, stream>>>(x, ws);
  k4_mlp<<<256, 512, 0, stream>>>(ws);
  k5_out<<<256, 576, 0, stream>>>(ws, (u16*)d_out);
}
```

```cpp
#include <hip/hip_runtime.h>
#include <hip/hip_bf16.h>
#include <stdint.h>

typedef unsigned short u16;
typedef short bf16x8 __attribute__((ext_vector_type(8)));
typedef short s16x4 __attribute__((ext_vector_type(4)));
typedef float f32x4 __attribute__((ext_vector_type(4)));
typedef float f32x16 __attribute__((ext_vector_type(16)));
typedef unsigned u32x4 __attribute__((ext_vector_type(4)));
typedef unsigned u32x2 __attribute__((ext_vector_type(2)));
#define LAS __attribute__((address_space(3)))
#define GAS __attribute__((address_space(1)))
#define DI __device__ __forceinline__

constexpr int S = 2048, D = 576, NH = 32, DH = 18, F = 4608;
constexpr int KS = 18;

constexpr size_t MODP_OFF = 0;
constexpr size_t WQKVT_OFF = 0x10000;
constexpr size_t WOT_OFF = WQKVT_OFF + 8 * 14 * 18 * 1024;
constexpr size_t W1T_OFF = WOT_OFF + 36 * 18 * 1024;
constexpr size_t W2T_OFF = W1T_OFF + 8 * 648 * 1024;
constexpr size_t QLO_OFF = W2T_OFF + 8 * 648 * 1024;
constexpr size_t QHI_OFF = QLO_OFF + 32 * 64 * 1024;
constexpr int KVT = 2432;
constexpr int KVH = 64 * KVT;
constexpr size_t KV_OFF = QHI_OFF + 32 * 2048 * 4;
constexpr size_t OF_OFF = KV_OFF + (size_t)32 * KVH;
constexpr size_t X1_OFF = OF_OFF + (size_t)S * D * 2;
constexpr size_t X1G_OFF = X1_OFF + (size_t)S * D * 2;
constexpr size_t NF_OFF = X1G_OFF + (size_t)S * D * 2;
constexpr size_t STAT_OFF = NF_OFF + (size_t)S * D * 2;
constexpr size_t CSB_OFF = STAT_OFF + 64 * 4 * 64 * 4;
constexpr size_t CSBF_OFF = CSB_OFF + 6 * 2 * 4608 * 4;
constexpr size_t MODF_OFF = CSBF_OFF + 2 * 4608 * 4;
constexpr size_t PART_OFF = CSB_OFF + 0x50000;
constexpr size_t WS_END = PART_OFF + (size_t)8 * S * D * 2;

#define TSDECL
#define TS(kid, i)
#define TSEND(kid)
#define PROFARG
#define PROFPASS
template <int I> struct IC { static constexpr int v = I; };
template <int B, int E, class Fn> __device__ __forceinline__ void sfor(Fn&& f) {
  if constexpr (B < E) { f(IC<B>{}); sfor<B + 1, E>(f); }
}
#define LAM(x) [&](auto x) __attribute__((always_inline))
DI float bf2f(unsigned v) { return __builtin_bit_cast(float, v << 16); }
DI float bflo(unsigned v) { return __builtin_bit_cast(float, v << 16); }
DI float bfhi(unsigned v) { return __builtin_bit_cast(float, v & 0xffff0000u); }
typedef __bf16 bf16x2_t __attribute__((ext_vector_type(2)));
typedef float f32x2 __attribute__((ext_vector_type(2)));
DI unsigned cvtpk(float lo, float hi) {
  return __builtin_bit_cast(unsigned, __builtin_convertvector(f32x2{lo, hi}, bf16x2_t));
}
DI float rbf(float x) { return bflo(cvtpk(x, x)); }
DI f32x4 mfma16(u32x4 a, u32x4 b, f32x4 c) {
  return __builtin_amdgcn_mfma_f32_16x16x32_bf16(__builtin_bit_cast(bf16x8, a), __builtin_bit_cast(bf16x8, b), c, 0, 0, 0);
}
DI f32x16 mfma32(u32x4 a, u32x4 b, f32x16 c) {
  return __builtin_amdgcn_mfma_f32_32x32x16_bf16(__builtin_bit_cast(bf16x8, a), __builtin_bit_cast(bf16x8, b), c, 0, 0, 0);
}
DI void glds16(const void* g, unsigned lds_base) {
  unsigned sv;
  asm volatile("s_mov_b32 %0, m0\n\ts_mov_b32 m0, %2\n\ts_nop 0\n\tglobal_load_lds_dwordx4 %1, off\n\ts_mov_b32 m0, %0"
               : "=&s"(sv) : "v"(g), "s"(lds_base) : "memory");
}
typedef __amdgpu_buffer_rsrc_t rsrc_t;
DI rsrc_t ws_rsrc(const char* ws) { return __builtin_amdgcn_make_buffer_rsrc((void*)ws, (short)0, (int)WS_END, 0x00020000); }
template <int AUX> DI void bst16(rsrc_t r, unsigned voff, unsigned soff, u32x4 v) { __builtin_amdgcn_raw_buffer_store_b128(v, r, voff + soff, 0, AUX); }
template <int AUX> DI void bst8(rsrc_t r, unsigned voff, unsigned soff, u32x2 v) { __builtin_amdgcn_raw_buffer_store_b64(v, r, voff, soff, AUX); }
template <int AUX> DI void bst4(rsrc_t r, unsigned voff, unsigned soff, unsigned v) { __builtin_amdgcn_raw_buffer_store_b32(v, r, voff, soff, AUX); }
DI u32x4 bld16(rsrc_t r, unsigned voff, unsigned soff) { return __builtin_amdgcn_raw_buffer_load_b128(r, voff, soff, 0); }
#define AUX_RT 16
#define AUX_K0L 16
#define AUX_K0G 16
#define AUX_O 16
#define AUX_PART 16
#define WAIT_VM(n) asm volatile("s_waitcnt vmcnt(" #n ")" ::: "memory")
#define SBAR() __builtin_amdgcn_sched_barrier(0)
DI float silu_f(float x) { return x * __builtin_amdgcn_rcpf(1.f + __builtin_amdgcn_exp2f(-1.4426950408889634f * x)); }
typedef short v4i16_t __attribute__((ext_vector_type(4)));
DI u32x2 tr_read(const LAS char* p) {
  return __builtin_bit_cast(u32x2, __builtin_amdgcn_ds_read_tr16_b64_v4i16((LAS v4i16_t*)p));
}
template <int CTRL> DI unsigned dppu(unsigned v) { return __builtin_amdgcn_update_dpp(0u, v, CTRL, 0xF, 0xF, true); }
DI unsigned dpp_xor1(unsigned v) { return dppu<0xB1>(v); }
template <int CTRL> DI float dppf(float v) { return __builtin_bit_cast(float, dppu<CTRL>(__builtin_bit_cast(unsigned, v))); }
DI float sum16(float v) { v += dppf<0xB1>(v); v += dppf<0x4E>(v); v += dppf<0x141>(v); v += dppf<0x140>(v); return v; }
DI u32x4 lds128(const LAS char* p) { return *(const LAS u32x4*)p; }

DI int frag_index(int T, int s) {
  return T < 20 ? (T / 5) * 90 + s * 5 + (T % 5) : 360 + ((T - 20) >> 2) * 72 + s * 4 + ((T - 20) & 3);
}

DI int frag_index1(int T, int s) {
  if (T < 16) { const int w = T >> 1; return (w < 4 ? 90 * w : 360 + 72 * (w - 4)) + 2 * s + (T & 1); }
  if (T < 28) { const int w = (T - 16) / 3; return 90 * w + 36 + 3 * s + (T - 16) % 3; }
  const int w = 4 + ((T - 28) >> 1);
  return 360 + 72 * (w - 4) + 36 + 2 * s + (T & 1);
}

constexpr int K0_MOD = 216, K0_LN = 128, K0_QKV_BLKS = 144;
__global__ void __launch_bounds__(256) k0_prep(const u16* __restrict__ c, const u16* __restrict__ wmod, const u16* __restrict__ wqkv,
                                               const u16* __restrict__ x, char* __restrict__ ws) {
  __shared__ __attribute__((aligned(16))) char lds[18432];
  const int tid = threadIdx.x, lane = tid & 63, w = __builtin_amdgcn_readfirstlane(tid >> 6);
  int b = blockIdx.x;
  TSDECL; TS(0, 0);
  if (b < K0_MOD) {
    const int cc = b % 54, ks = b / 54;
    const int ch = tid & 7, rg = tid >> 3;
    float acc[8];
#pragma unroll
    for (int j = 0; j < 8; ++j) acc[j] = 0.f;
    u32x4 wv[5];
    float sv[5];
    unsigned cr[5];
#pragma unroll
    for (int i = 0; i < 5; ++i) {
      const int r = rg + 32 * i, k = ks * 144 + (r < 144 ? r : 0);
      cr[i] = c[k];
      wv[i] = *(const u32x4*)(wmod + (size_t)k * 3456 + cc * 64 + ch * 8);
    }
    SBAR();
#pragma unroll
    for (int i = 0; i < 5; ++i) sv[i] = rbf(silu_f(bf2f(cr[i]))) * (rg + 32 * i < 144 ? 1.f : 0.f);
#pragma unroll
    for (int i = 0; i < 5; ++i)
#pragma unroll
      for (int j = 0; j < 4; ++j) {
        acc[2 * j] += sv[i] * bflo(wv[i][j]);
        acc[2 * j + 1] += sv[i] * bfhi(wv[i][j]);
      }
#pragma unroll
    for (int j = 0; j < 8; ++j) {
      acc[j] += __shfl_xor(acc[j], 8);
      acc[j] += __shfl_xor(acc[j], 16);
      acc[j] += __shfl_xor(acc[j], 32);
    }
    float* red = (float*)lds;
    if (lane < 8)
#pragma unroll
      for (int j = 0; j < 8; ++j) red[w * 64 + lane * 8 + j] = acc[j];
    __syncthreads();
    if (tid < 64) ((float*)(ws + MODP_OFF))[ks * 3456 + cc * 64 + tid] = red[tid] + red[64 + tid] + red[128 + tid] + red[192 + tid];
    TS(0, 1); TSEND(0);
    return;
  }
  b -= K0_MOD;
  if (b < K0_LN) {
    const int tok = b * 16 + w * 4 + (lane >> 4), p = lane & 15;
    const u16* xp = x + (size_t)tok * D + 4 * p;
    u32x2 xv[9];
#pragma unroll
    for (int i = 0; i < 9; ++i) xv[i] = *(const u32x2*)(xp + 64 * i);
    float sm = 0.f;
#pragma unroll
    for (int i = 0; i < 9; ++i) sm += (bflo(xv[i][0]) + bfhi(xv[i][0])) + (bflo(xv[i][1]) + bfhi(xv[i][1]));
    const float mean = sum16(sm) * (1.f / D);
    float q = 0.f;
#pragma unroll
    for (int i = 0; i < 9; ++i)
#pragma unroll
      for (int j = 0; j < 2; ++j) { float a0 = bflo(xv[i][j]) - mean, a1 = bfhi(xv[i][j]) - mean; q += a0 * a0; q += a1 * a1; }
    const float rstd = __builtin_amdgcn_rsqf(sum16(q) * (1.f / D) + 1e-5f);
    LAS char* dst = (LAS char*)lds + (p >> 3) * 1024 + ((p >> 1) & 3) * 256 + (tok & 15) * 16 + (p & 1) * 8;
#pragma unroll
    for (int i = 0; i < 9; ++i) {
      u32x2 o = {cvtpk((bflo(xv[i][0]) - mean) * rstd, (bfhi(xv[i][0]) - mean) * rstd),
                 cvtpk((bflo(xv[i][1]) - mean) * rstd, (bfhi(xv[i][1]) - mean) * rstd)};
      *(LAS u32x2*)(dst + i * 2048) = o;
    }
    __syncthreads();
    const rsrc_t rs0 = ws_rsrc(ws);
#pragma unroll
    for (int i = 0; i < 5; ++i) {
      const int s = w + 4 * i;
      if (s < 18) bst16<AUX_K0L>(rs0, lane * 16, (unsigned)NF_OFF + (b >> 2) * 73728 + s * 4096 + (b & 3) * 1024, lds128((const LAS char*)lds + s * 1024 + lane * 16));
    }
    TS(0, 1); TSEND(0);
    return;
  }
  b -= K0_LN;
  {
    const int s = b % 18, hq = b / 18;
    constexpr int PITCH = 448;
    u32x4 raw[4];
#pragma unroll
    for (int i = 0; i < 4; ++i) {
      const int e = min(tid + 256 * i, 863), row = e / 27, pc = e % 27;
      raw[i] = *(const u32x4*)(wqkv + (size_t)(32 * s + row) * 1728 + (pc / 9) * 576 + hq * 72 + (pc % 9) * 8);
    }
#pragma unroll
    for (int i = 0; i < 4; ++i) {
      const int e = min(tid + 256 * i, 863), row = e / 27, pc = e % 27;
      *(LAS u32x4*)((LAS char*)lds + row * PITCH + pc * 16) = raw[i];
    }
    __syncthreads();
    const int g = lane >> 4, r = lane & 15;
    const rsrc_t rs0 = ws_rsrc(ws);
#pragma unroll
    for (int i = 0; i < 4; ++i) {
      const int tile = w + 4 * i;
      if (tile < 14) {
        int col; bool ok = true;
        if (tile < 12) col = (tile >> 2) * 72 + (tile & 3) * 18 + r;
        else if (tile == 12) col = (r >> 3) * 72 + ((r >> 1) & 3) * 18 + 16 + (r & 1);
        else { ok = r < 8; col = 144 + ((r >> 1) & 3) * 18 + 16 + (r & 1); }
        const LAS u16* p = (const LAS u16*)((LAS char*)lds + 8 * g * PITCH) + col;
        u32x4 o;
#pragma unroll
        for (int j = 0; j < 4; ++j) o[j] = ok ? ((unsigned)p[(2 * j) * (PITCH / 2)] | ((unsigned)p[(2 * j + 1) * (PITCH / 2)] << 16)) : 0u;
        bst16<AUX_K0G>(rs0, lane * 16, (unsigned)WQKVT_OFF + ((hq * 14 + tile) * 18 + s) * 1024, o);
      }
    }
    TS(0, 1); TSEND(0);
  }
}

DI f32x4 mod4(const float* __restrict__ modp, int col) {
  const f32x4 a = *(const f32x4*)(modp + col), b = *(const f32x4*)(modp + 3456 + col), c = *(const f32x4*)(modp + 2 * 3456 + col),
              d = *(const f32x4*)(modp + 3 * 3456 + col);
  return f32x4{rbf(a[0] + b[0] + c[0] + d[0]), rbf(a[1] + b[1] + c[1] + d[1]), rbf(a[2] + b[2] + c[2] + d[2]), rbf(a[3] + b[3] + c[3] + d[3])};
}
DI f32x4 gain4(f32x4 v) { return f32x4{rbf(1.f + v[0]), rbf(1.f + v[1]), rbf(1.f + v[2]), rbf(1.f + v[3])}; }

constexpr float QSCALE = 0.23570226039551584f * 1.4426950408889634f;
DI u32x4 modulate8(u32x4 v, const f32x4& g0, const f32x4& g1, const f32x4& s0, const f32x4& s1) {
  return u32x4{cvtpk(bflo(v[0]) * g0[0] + s0[0], bfhi(v[0]) * g0[1] + s0[1]), cvtpk(bflo(v[1]) * g0[2] + s0[2], bfhi(v[1]) * g0[3] + s0[3]),
               cvtpk(bflo(v[2]) * g1[0] + s1[0], bfhi(v[2]) * g1[1] + s1[1]), cvtpk(bflo(v[3]) * g1[2] + s1[2], bfhi(v[3]) * g1[3] + s1[3])};
}
#define AUX_K1 16
#define RAWBAR() asm volatile("s_barrier" ::: "memory")
#define LDSBAR() asm volatile("s_waitcnt lgkmcnt(0)\n\ts_barrier" ::: "memory")
DI void swap16(unsigned& a, unsigned& b) { const auto r = __builtin_amdgcn_permlane16_swap(a, b, false, false); a = r[0]; b = r[1]; }
DI void swap32(unsigned& a, unsigned& b) { const auto r = __builtin_amdgcn_permlane32_swap(a, b, false, false); a = r[0]; b = r[1]; }
constexpr int K1_WIN = 12;
constexpr unsigned OOB = 0x40000000u;
template <bool N0, bool N1>
DI void qkv_gemm(rsrc_t rs, unsigned vo, unsigned so, u32x4 (&a)[K1_WIN], const LAS char* b0, f32x4 (&acc)[2][4]) {
  constexpr int WIN = K1_WIN;
  const f32x4 z4 = {0.f, 0.f, 0.f, 0.f};
#pragma unroll
  for (int j = 0; j < 2; ++j)
#pragma unroll
    for (int t = 0; t < 4; ++t) acc[j][t] = z4;
  u32x4 bf[2][4];
#pragma unroll
  for (int t = 0; t < 4; ++t) bf[0][t] = lds128(b0 + t * 1024);
#pragma unroll 1
  for (int it = 0; it < 3; ++it) {
    const unsigned vn = it < 2 ? vo : OOB;
#pragma unroll
    for (int u = 0; u < 6; ++u) {
      const LAS char* bn = b0 + (u + 1) * 4096;
#pragma unroll
      for (int t = 0; t < 4; ++t) bf[(u + 1) & 1][t] = lds128(bn + t * 1024);
      SBAR();
#pragma unroll
      for (int j = 0; j < 2; ++j) {
#pragma unroll
        for (int t = 0; t < 4; ++t)
          acc[j][t] = (j == 0 ? N0 : N1) ? mfma16(bf[u & 1][t], a[2 * u + j], acc[j][t]) : mfma16(a[2 * u + j], bf[u & 1][t], acc[j][t]);
        SBAR();
        a[2 * u + j] = bld16(rs, vn, so + (j * 18 + 6 + u) * 1024);
        SBAR();
      }
    }
    b0 += 6 * 4096; so += 6 * 1024;
  }
}
template <bool GEMM, bool N0, bool N1>
DI void qkv_body(rsrc_t rs, unsigned vo, unsigned so, const u32x4 (&xv)[9], LAS char* hb, const LAS float* modv, int w, int lane,
                 f32x4 (&acc)[2][4] PROFARG) {
  u32x4 a[K1_WIN];
  const LAS float* gp = modv + (lane >> 4) * 8;
  f32x4 gs[3][4];
#pragma unroll
  for (int k = 0; k < 3; ++k) {
    const LAS float* p = gp + (k < 2 ? 2 * w + k : 16 + (w >> 2)) * 32;
    gs[k][0] = *(const LAS f32x4*)p; gs[k][1] = *(const LAS f32x4*)(p + 4);
    gs[k][2] = *(const LAS f32x4*)(p + 576); gs[k][3] = *(const LAS f32x4*)(p + 580);
  }
  SBAR();
  sfor<0, 9>(LAM(i_) {
    constexpr int i = i_.v, l0 = i < 3 ? 2 * i : i + 3, l1 = i < 3 ? 2 * i + 2 : i + 4;
    if constexpr (GEMM) sfor<l0, l1>(LAM(l_) { constexpr int l = l_.v; a[l] = bld16(rs, vo, so + ((l & 1) * 18 + (l >> 1)) * 1024); });
    SBAR();
    const u32x4 m = modulate8(xv[i], gs[i >> 2][0], gs[i >> 2][1], gs[i >> 2][2], gs[i >> 2][3]);
    *(LAS u32x4*)(hb + (i < 8 ? 8 * w + i : 64 + w) * 1024 + lane * 16) = m;
    SBAR();
  });
  TS(1, 2);
  LDSBAR();
  TS(1, 3);
  if constexpr (GEMM) qkv_gemm<N0, N1>(rs, vo, so, a, hb + lane * 16, acc);
}
__global__ void __launch_bounds__(512, 2) k1_qkv(char* __restrict__ ws) {
  __shared__ __attribute__((aligned(16))) char lds[73728 + 4608];
  const int tid = threadIdx.x, lane = tid & 63, w = __builtin_amdgcn_readfirstlane(tid >> 6);
  const int xcd = blockIdx.x & 7, idx = blockIdx.x >> 3;
  const int rb = (xcd >> 1) * 8 + (idx & 7), hq = (xcd & 1) * 4 + (idx >> 3);
  LAS char* hb = (LAS char*)lds;
  LAS float* modv = (LAS float*)(hb + 73728);
  const rsrc_t rs = ws_rsrc(ws);
  const unsigned vo = lane * 16;
  TSDECL; TS(1, 0);
  const bool domod = tid < 288;
  const unsigned mo = domod ? (unsigned)MODP_OFF + (tid < 144 ? D + 4 * tid : 4 * (tid - 144)) * 4 : OOB;
  u32x4 mp[4];
#pragma unroll
  for (int i = 0; i < 4; ++i) mp[i] = bld16(rs, mo, i * 3456 * 4);
  TS(1, 6);
  SBAR(); RAWBAR(); SBAR();
  TS(1, 7);
  u32x4 xv[9];
  {
    const unsigned no = (unsigned)NF_OFF + rb * 73728;
#pragma unroll
    for (int i = 0; i < 8; ++i) xv[i] = bld16(rs, vo, no + (8 * w + i) * 1024);
    xv[8] = bld16(rs, vo, no + (64 + w) * 1024);
  }
  SBAR();
  TS(1, 8);
  {
    const f32x4 ms = __builtin_bit_cast(f32x4, mp[0]) + __builtin_bit_cast(f32x4, mp[1]) + __builtin_bit_cast(f32x4, mp[2]) + __builtin_bit_cast(f32x4, mp[3]);
    const f32x4 mv = {rbf(ms[0]), rbf(ms[1]), rbf(ms[2]), rbf(ms[3])};
    *(LAS f32x4*)(domod ? (LAS char*)(modv + 4 * tid) : hb + 16 * tid) = tid < 144 ? gain4(mv) : mv;
  }
  LDSBAR();
  TS(1, 1);
  const unsigned so = (unsigned)WQKVT_OFF + (hq * 14 + 2 * w) * 18 * 1024;
  const int g = lane >> 4, c = lane & 15;
  f32x4 acc[2][4];
  if (w == 7) {
    qkv_body<false, false, false>(rs, vo, so, xv, hb, modv, w, lane, acc PROFPASS);
    const int i = blockIdx.x * 64 + lane;
    if (i < 864) {
      const f32x4 v = mod4((const float*)(ws + MODP_OFF), 4 * i);
      const int vec = i / 144;
      *(f32x4*)(ws + MODF_OFF + 16 * i) = (vec == 1 || vec == 4) ? gain4(v) : v;
    }
    TS(1, 4); TSEND(1);
    return;
  }
  if (w < 4) qkv_body<true, false, false>(rs, vo, so, xv, hb, modv, w, lane, acc PROFPASS);
  else if (w < 6) qkv_body<true, true, true>(rs, vo, so, xv, hb, modv, w, lane, acc PROFPASS);
  else qkv_body<true, false, true>(rs, vo, so, xv, hb, modv, w, lane, acc PROFPASS);
  TS(1, 4);
  const float sc = w < 2 || (w == 6 && g < 2) ? QSCALE : 1.f;
  unsigned o[2][4][2];
#pragma unroll
  for (int j = 0; j < 2; ++j) {
    const float f = (w == 6 && j == 1) ? 1.f : sc;
#pragma unroll
    for (int t = 0; t < 4; ++t) {
      o[j][t][0] = cvtpk(acc[j][t][0] * f, acc[j][t][1] * f);
      o[j][t][1] = cvtpk(acc[j][t][2] * f, acc[j][t][3] * f);
    }
  }
  const unsigned kvo = (unsigned)KV_OFF + rb * 2 * KVT;
  if (w < 4) {
#pragma unroll
    for (int j = 0; j < 2; ++j) {
      const int h = 4 * hq + 2 * (w & 1) + j;
      const unsigned base = w < 2 ? (unsigned)QLO_OFF + (h * 64 + rb * 2) * 1024 : kvo + h * KVH, st = w < 2 ? 1024 : KVT;
#pragma unroll
      for (int p = 0; p < 2; ++p) {
        swap16(o[j][2 * p][0], o[j][2 * p + 1][0]); swap16(o[j][2 * p][1], o[j][2 * p + 1][1]);
        bst16<AUX_K1>(rs, vo, base + p * st, u32x4{o[j][2 * p][0], o[j][2 * p][1], o[j][2 * p + 1][0], o[j][2 * p + 1][1]});
      }
    }
  } else {
#pragma unroll
    for (int j = 0; j < 2; ++j) {
      unsigned vv;
      unsigned base = kvo + 1152;
      if (w < 6) { base += (4 * hq + 2 * (w & 1) + j) * KVH; vv = c * 16; }
      else if (j == 0) continue;
      else {
        const int h = c < 8 ? c >> 1 : c & 3, d = c < 8 ? 16 + (c & 1) : c < 12 ? 18 : 19;
        vv = (4 * hq + h) * KVH + d * 16;
        if (c >= 8) {
          const unsigned cv = c < 12 ? 0x3F803F80u : 0u;
#pragma unroll
          for (int t = 0; t < 4; ++t) { o[1][t][0] = cv; o[1][t][1] = cv; }
        }
      }
      vv += (g >> 1) * 640 + (g & 1) * 320;
#pragma unroll
      for (int p = 0; p < 2; ++p) {
        swap32(o[j][2 * p][0], o[j][2 * p + 1][0]); swap32(o[j][2 * p][1], o[j][2 * p + 1][1]);
        bst16<AUX_K1>(rs, vv, base + p * KVT, u32x4{o[j][2 * p][0], o[j][2 * p][1], o[j][2 * p + 1][0], o[j][2 * p + 1][1]});
      }
    }
    if (w == 6) {
#pragma unroll
      for (int e = 0; e < 2; ++e) {
        const int h = 4 * hq + 2 * (g & 1) + e;
        const unsigned v0 = g < 2 ? (unsigned)QHI_OFF + (h * 2048 + rb * 64 + c) * 4 : kvo + h * KVH + 1024 + c * 4;
        const unsigned v1 = v0 + (g < 2 ? 128 : KVT);
#pragma unroll
        for (int t = 0; t < 4; ++t) bst4<AUX_K1>(rs, (t < 2 ? v0 : v1) + (t & 1) * 64, 0, o[0][t][e]);
      }
    }
  }
  TS(1, 5); TSEND(1);
}

#define ATT_ORDER 0
#define ATT_VAR 0
DI float ex2(float x) {
  return __builtin_amdgcn_exp2f(x);
}
typedef short s16x4v __attribute__((ext_vector_type(4)));
DI f32x16 mfma32k8(u32x4 a, u32x4 b, f32x16 c) {
  return mfma32(a, b, c);
}
DI f32x16 mfmapv(u32x4 a, u32x4 b, f32x16 c) {
  return mfma32(a, b, c);
}
DI u32x4 ldsv(const LAS char* p, u32x4 old, bool act) {
  return *(const LAS u32x4*)p;
}
#define ATT_G(P, i0, S, j0) \
  e0 = ex2(S[j0]); e1 = ex2(S[j0 + 1]); e2 = ex2(S[j0 + 2]); e3 = ex2(S[j0 + 3]); \
  SBAR(); P[i0] = cvtpk(e0, e1); P[i0 + 1] = cvtpk(e2, e3); SBAR();
DI void attn_step(const LAS char* kp, const LAS char* hp, const LAS char* vp, int off, u32x4& ka, u32x4& kb, u32x4& v0, u32x4& v1,
                  const u32x4 q0, const u32x4 q1, const f32x16& cin, f32x16& sc, f32x16& sn, u32x4& p1, f32x16& o, bool vact) {
  u32x4 p0;
  float e0, e1, e2, e3;
  ATT_G(p0, 0, sc, 0)
  o = mfmapv(v1, p1, o);
  SBAR();
  v1 = ldsv(vp + off + 640, v1, vact);
  ATT_G(p0, 2, sc, 4)
  sn = mfma32k8(kb, q1, sn);
  SBAR();
  kb[0] = *(const LAS unsigned*)(hp + off + 2 * KVT);
  ATT_G(p1, 0, sc, 8)
  o = mfmapv(v0, p0, o);
  SBAR();
  v0 = ldsv(vp + off + KVT, v0, vact);
  ATT_G(p1, 2, sc, 12)
  sc = mfma32(ka, q0, cin);
  SBAR();
  ka = lds128(kp + off + 3 * KVT);
}
template <bool SUBMAX, bool STAGED>
DI void attn_all(const LAS char* kv, u32x4 q0, u32x4 q1, float negm, int lane, f32x16& o) {
  const LAS char* kp = kv + lane * 16;
  const LAS char* hp = kv + 1024 + (lane & 31) * 4;
  const LAS char* vp = kv + 1152 + (lane >> 5) * 320 + (lane & 31) * 16;
  f32x16 cin;
#pragma unroll
  for (int i = 0; i < 16; ++i) cin[i] = SUBMAX ? negm : 0.f;
  u32x4 ka = lds128(kp);
  u32x4 kb = {*(const LAS unsigned*)(hp), 0u, 0u, 0u};
  f32x16 sa = mfma32(ka, q0, cin), sb;
  sa = mfma32k8(kb, q1, sa);
  ka = lds128(kp + KVT);
  sb = mfma32(ka, q0, cin);
  ka = lds128(kp + 2 * KVT);
  kb[0] = *(const LAS unsigned*)(hp + KVT);
  const bool vact = (lane & 31) < 20;
  const u32x4 zz = {0u, 0u, 0u, 0u};
  u32x4 v0 = ldsv(vp, zz, vact), v1 = zz, p1 = zz;
#pragma unroll 1
  for (int c = 0; c < 8; ++c) {
    if (STAGED) {
      if (c == 1 || c == 2) asm volatile("s_barrier" ::: "memory");
    }
#pragma unroll
    for (int u = 0; u < 8; u += 2) {
      attn_step(kp, hp, vp, u * KVT, ka, kb, v0, v1, q0, q1, cin, sa, sb, p1, o, vact);
      attn_step(kp, hp, vp, (u + 1) * KVT, ka, kb, v0, v1, q0, q1, cin, sb, sa, p1, o, vact);
    }
    kp += 8 * KVT; hp += 8 * KVT; vp += 8 * KVT;
  }
  o = mfmapv(v1, p1, o);
}
#define HELP_PRIO 0
#define HELP_MODE 2
template <int NU, bool ISW1>
DI void retile_units(const u16* __restrict__ w1, const u16* __restrict__ w2, const u16* __restrict__ wo, char* __restrict__ ws,
                     LAS char* stage, int job, int lane) {
  if (!ISW1) __builtin_amdgcn_s_setprio(2);
  u32x4 raw[NU][4];
  const int rr = lane >> 3, cc = (lane & 7) * 8;
#pragma unroll
  for (int u = 0; u < NU; ++u) {
    if (ISW1) {
      const int cgp = job / 6, s = (job % 6) * 3 + u;
#pragma unroll
      for (int i = 0; i < 4; ++i) raw[u][i] = *(const u32x4*)(w1 + (size_t)(32 * s + 8 * i + rr) * F + cgp * 64 + cc);
    } else {
      const int un = 3 * job + u;
      const bool isw2 = un < 1296;
      const int sg = un % 144, v = un - 1296, s = v % 18;
      const int n0 = isw2 ? (un / 144) * 64 : (v / 18) * 64;
#pragma unroll
      for (int i = 0; i < 4; ++i) {
        const int r = (8 * i + rr) & 15, g = r >> 2, j = 4 * (i >> 1) + (r & 3), ci = 4 * s + g, G = ci / 9, r9 = ci % 9;
        const int ko = r9 < 8 ? (4 * G + (r9 >> 1)) * 18 + 8 * (r9 & 1) + j : (4 * G + (j >> 1)) * 18 + 16 + (j & 1);
        const int k = isw2 ? 32 * sg + 8 * i + rr : ko;
        raw[u][i] = *(const u32x4*)((isw2 ? w2 : wo) + (size_t)k * D + n0 + cc);
      }
    }
  }
  SBAR();
  const int g = lane >> 4, c = lane & 15;
  u32x4 bv[ISW1 ? NU : 1];
  if (ISW1) {
#pragma unroll
    for (int u = 0; u < NU; ++u) {
      const float* mp = (const float*)(ws + MODF_OFF) + ((c & 1) ? 3 : 4) * D + 32 * ((job % 6) * 3 + u) + 8 * g;
      const f32x4 lo = *(const f32x4*)mp, hi = *(const f32x4*)(mp + 4);
      bv[u] = c < 2 ? u32x4{cvtpk(lo[0], lo[1]), cvtpk(lo[2], lo[3]), cvtpk(hi[0], hi[1]), cvtpk(hi[2], hi[3])} : u32x4{0u, 0u, 0u, 0u};
    }
  }
  const rsrc_t rs = ws_rsrc(ws);
  f32x4 acc[4];
#pragma unroll
  for (int i = 0; i < 4; ++i) acc[i] = f32x4{0.f, 0.f, 0.f, 0.f};
  const LAS char* rp = stage + (4 * g + (c >> 2)) * 128 + (lane & 3) * 8;
#pragma unroll
  for (int u = 0; u < NU; ++u) {
    u32x2 lo[4], hi[4];
    LAS char* sp = stage + (rr & 3) * 128 + (lane & 7) * 16;
    if (ISW1) {
      if (rr < 4) {
#pragma unroll
        for (int i = 0; i < 4; ++i) *(LAS u32x4*)(sp + i * 512) = raw[u][i];
      }
    } else {
      *(LAS u32x4*)(stage + lane * 16) = raw[u][0];
      *(LAS u32x4*)(stage + 1024 + lane * 16) = raw[u][1];
    }
#pragma unroll
    for (int i = 0; i < 4; ++i) lo[i] = tr_read(rp + i * 32);
    asm volatile("s_waitcnt lgkmcnt(0)" ::: "memory");
    if (ISW1) {
      if (rr >= 4) {
#pragma unroll
        for (int i = 0; i < 4; ++i) *(LAS u32x4*)(sp + i * 512) = raw[u][i];
      }
    } else {
      *(LAS u32x4*)(stage + lane * 16) = raw[u][2];
      *(LAS u32x4*)(stage + 1024 + lane * 16) = raw[u][3];
    }
#pragma unroll
    for (int i = 0; i < 4; ++i) hi[i] = tr_read(rp + i * 32);
    asm volatile("s_waitcnt lgkmcnt(0)" ::: "memory");
#pragma unroll
    for (int i = 0; i < 4; ++i) {
      const u32x4 fragv = {lo[i][0], lo[i][1], hi[i][0], hi[i][1]};
      size_t off;
      if (ISW1) {
        const int Tn = (job / 6) * 4 + i, s = (job % 6) * 3 + u;
        off = W1T_OFF + ((size_t)(Tn / 36) * 648 + frag_index1(Tn % 36, s)) * 1024;
        acc[i] = mfma16(fragv, bv[u], acc[i]);
      } else {
        const int un = 3 * job + u;
        if (un < 1296) {
          const int sg = un % 144;
          off = W2T_OFF + ((size_t)(sg / 18) * 648 + frag_index((un / 144) * 4 + i, sg % 18)) * 1024;
        } else {
          const int v = un - 1296;
          off = WOT_OFF + ((size_t)((v / 18) * 4 + i) * 18 + v % 18) * 1024;
        }
      }
      bst16<AUX_RT>(rs, lane * 16, (unsigned)off, fragv);
    }
    if (u == 0 && !ISW1) __builtin_amdgcn_s_setprio(0);
  }
  if (ISW1 && c < 2) {
#pragma unroll
    for (int i = 0; i < 4; ++i)
      *(f32x4*)(ws + CSB_OFF + ((size_t)((job % 6) * 2 + c) * F + ((job / 6) * 4 + i) * 16 + 4 * g) * 4) = acc[i];
  }
}
__global__ void __launch_bounds__(768, 3) k2_attn(const u16* __restrict__ w1, const u16* __restrict__ w2, const u16* __restrict__ wo,
                                                  char* __restrict__ ws) {
  __shared__ __attribute__((aligned(16))) char lds[163840];
  const int tid = threadIdx.x, lane = tid & 63, w = __builtin_amdgcn_readfirstlane(tid >> 6);
  const int xcd = blockIdx.x & 7, idx = blockIdx.x >> 3, h = 4 * xcd + (idx & 3), qb = idx >> 2;
  TSDECL; TS(2, 0);
  if (w >= 8) {
    __builtin_amdgcn_s_setprio(HELP_PRIO);
    {
      const char* src = ws + KV_OFF + (size_t)h * KVH + (w - 8) * 2048 + lane * 16;
      const unsigned lb = (unsigned)(size_t)(LAS char*)lds + (w - 8) * 2048;
      asm volatile("s_barrier" ::: "memory");
#pragma unroll
      for (int r = 4; r < 12; ++r) { glds16(src + r * 8192, lb + r * 8192); glds16(src + r * 8192 + 1024, lb + r * 8192 + 1024); }
      asm volatile("s_waitcnt vmcnt(12)\n\ts_barrier" ::: "memory");
#pragma unroll
      for (int r = 12; r < 19; ++r) { glds16(src + r * 8192, lb + r * 8192); glds16(src + r * 8192 + 1024, lb + r * 8192 + 1024); }
      asm volatile("s_waitcnt vmcnt(0)\n\ts_barrier" ::: "memory");
    }
    TS(2, 1);
    LAS char* stage = (LAS char*)lds + KVH + (w - 8) * 2048;
    const int job = blockIdx.x * 2 + (w & 1);
    if (w < 10) { if (job < 432) retile_units<3, true>(w1, w2, wo, ws, stage, job, lane); }
    else if (job < 486) retile_units<3, false>(w1, w2, wo, ws, stage, job, lane);
    TS(2, 2); TSEND(2);
    return;
  }
  const int qt = qb * 8 + w;
  u32x4 q0 = *(const u32x4*)(ws + QLO_OFF + ((size_t)h * 64 + qt) * 1024 + lane * 16);
  unsigned qh = *(const unsigned*)(ws + QHI_OFF + ((size_t)h * 2048 + qt * 32 + (lane & 31)) * 4);
  const char* src = ws + KV_OFF + (size_t)h * KVH + w * 1024 + lane * 16;
  const unsigned lb = (unsigned)(size_t)(LAS char*)lds + w * 1024;
#pragma unroll
  for (int r = 0; r < 4; ++r) glds16(src + r * 8192, lb + r * 8192);
  asm volatile("" : "+v"(q0), "+v"(qh));
  WAIT_VM(0);
  const u32x4 q1 = {lane < 32 ? qh : 0u, 0u, 0u, 0u};
  const LAS char* kv = (const LAS char*)lds;
  f32x16 o;
#pragma unroll
  for (int i = 0; i < 16; ++i) o[i] = 0.f;
  asm volatile("s_barrier" ::: "memory");
  TS(2, 1);
  attn_all<false, true>(kv, q0, q1, 0.f, lane, o);
  TS(2, 5);
  float l = __shfl(o[10], lane & 31);
  bool bad = !(l >= 7.8886091e-31f && l <= 1.3292280e36f);
#pragma unroll
  for (int i = 0; i < 10; ++i) bad |= !(__builtin_fabsf(o[i]) <= 3.0e38f) && (i < 8 || lane < 32);
  if (__builtin_expect(__any(bad), 0) && ATT_VAR < 3) {
    float m = -3.0e38f;
    const LAS char* kp = kv + lane * 16;
    const LAS char* hp = kv + 1024 + (lane & 31) * 4;
    f32x16 z;
#pragma unroll
    for (int i = 0; i < 16; ++i) z[i] = 0.f;
#pragma unroll 1
    for (int t = 0; t < 64; ++t) {
      const u32x4 ka = lds128(kp + t * KVT);
      const u32x4 kb = {*(const LAS unsigned*)(hp + t * KVT), 0u, 0u, 0u};
      f32x16 sc = mfma32(ka, q0, z);
      sc = mfma32(kb, q1, sc);
#pragma unroll
      for (int i = 0; i < 16; ++i) m = __builtin_fmaxf(m, sc[i]);
    }
    m = __builtin_fmaxf(m, __shfl_xor(m, 32));
#pragma unroll
    for (int i = 0; i < 16; ++i) o[i] = 0.f;
    attn_all<true, false>(kv, q0, q1, -m, lane, o);
    l = __shfl(o[10], lane & 31);
  }
  const float rl = 1.f / l;
  const int hf = lane >> 5, ci = 9 * (h >> 2) + 2 * (h & 3) + hf, ch = 9 * (h >> 2) + 8;
  const unsigned tokoff = ((lane >> 4) & 1) * 1024 + (lane & 15) * 16, ob = (unsigned)OF_OFF + qt * 36864;
  unsigned a0 = cvtpk(o[0] * rl, o[1] * rl), a1 = cvtpk(o[2] * rl, o[3] * rl), b0 = cvtpk(o[4] * rl, o[5] * rl), b1 = cvtpk(o[6] * rl, o[7] * rl);
  swap32(a0, b0); swap32(a1, b1);
  const rsrc_t rs = ws_rsrc(ws);
  bst16<AUX_O>(rs, (ci >> 2) * 2048 + (ci & 3) * 256 + tokoff, ob, u32x4{a0, a1, b0, b1});
  if (lane < 32) bst4<0>(rs, (ch >> 2) * 2048 + (ch & 3) * 256 + tokoff + (h & 3) * 4, ob, cvtpk(o[8] * rl, o[9] * rl));
  TS(2, 6); TSEND(2);
}

#define AUX_K3 16
#define AUX_K3G 16
#define K3_WIN 12
__global__ void __launch_bounds__(576) k3_wo(const u16* __restrict__ x, char* __restrict__ ws) {
  __shared__ __attribute__((aligned(16))) char lds[36 * 1024 + 9 * 64 * 4];
  const int tid = threadIdx.x, lane = tid & 63, w = __builtin_amdgcn_readfirstlane(tid >> 6);
  const int rb = (blockIdx.x & 7) * 8 + ((blockIdx.x >> 3) & 7), cg = blockIdx.x >> 6;
  TSDECL; TS(3, 0);
  const int g = lane >> 4, c = lane & 15;
  u32x4 cs[6];
  if (w == 8) {
    const rsrc_t rs = ws_rsrc(ws);
#pragma unroll
    for (int k = 0; k < 6; ++k) cs[k] = bld16(rs, lane < 9 ? (blockIdx.x * 9 + lane) * 16 : OOB, (unsigned)CSB_OFF + k * 2 * F * 4);
  }
  const unsigned lb = (unsigned)(size_t)(LAS char*)lds;
#pragma unroll
  for (int i = 0; i < 4; ++i) glds16(ws + OF_OFF + (size_t)rb * 36864 + (w * 4 + i) * 1024 + lane * 16, lb + (w * 4 + i) * 1024);
  const int T = cg * 9 + w;
  const u32x4* ap = (const u32x4*)(ws + WOT_OFF + (size_t)T * 18 * 1024) + lane;
  constexpr int W3 = K3_WIN;
  u32x4 a[18];
  SBAR();
#pragma unroll
  for (int s = 0; s < W3; ++s) a[s] = ap[s * 64];
  SBAR();
  asm volatile("s_waitcnt vmcnt(%0)\n\ts_barrier" :: "i"(W3) : "memory");
  TS(3, 1);
  if (w == 8 && lane < 9) {
    f32x4 sm = __builtin_bit_cast(f32x4, cs[0]);
#pragma unroll
    for (int k = 1; k < 6; ++k) sm += __builtin_bit_cast(f32x4, cs[k]);
    *(f32x4*)(ws + CSBF_OFF + (blockIdx.x * 9 + lane) * 16) = sm * -1.4426950408889634f;
  }
  u32x2 xr[2];
  f32x4 ga, gb2;
  f32x4 acc[2] = {f32x4{0.f, 0.f, 0.f, 0.f}, f32x4{0.f, 0.f, 0.f, 0.f}};
  const LAS char* bp = (const LAS char*)lds + lane * 16;
#pragma unroll
  for (int s = 0; s < 18; ++s) {
    if (s + W3 < 18) a[s + W3] = ap[(s + W3) * 64];
    if (s == 12) {
#pragma unroll
      for (int t = 0; t < 2; ++t) xr[t] = *(const u32x2*)(x + (size_t)(rb * 32 + t * 16 + c) * D + T * 16 + 4 * g);
    }
    if (s == 13) ga = *(const f32x4*)(ws + MODF_OFF + (2 * D + T * 16 + 4 * g) * 4);
    if (s == 14) gb2 = *(const f32x4*)(ws + MODF_OFF + (4 * D + T * 16 + 4 * g) * 4);
    const u32x4 b0 = lds128(bp + (s * 2) * 1024), b1 = lds128(bp + (s * 2 + 1) * 1024);
    SBAR();
    acc[0] = mfma16(a[s], b0, acc[0]);
    acc[1] = mfma16(a[s], b1, acc[1]);
    SBAR();
  }
  TS(3, 3);
  u32x4 xo;
  unsigned og[2][2];
  const rsrc_t rs3 = ws_rsrc(ws);
  LAS float* red = (LAS float*)(lds + 36 * 1024);
#pragma unroll
  for (int t = 0; t < 2; ++t) {
    const unsigned o0 = cvtpk(bflo(xr[t][0]) + ga[0] * acc[t][0], bfhi(xr[t][0]) + ga[1] * acc[t][1]);
    const unsigned o1 = cvtpk(bflo(xr[t][1]) + ga[2] * acc[t][2], bfhi(xr[t][1]) + ga[3] * acc[t][3]);
    xo[2 * t] = o0; xo[2 * t + 1] = o1;
    const float v0 = bflo(o0), v1 = bfhi(o0), v2 = bflo(o1), v3 = bfhi(o1);
    og[t][0] = cvtpk(v0 * gb2[0], v1 * gb2[1]); og[t][1] = cvtpk(v2 * gb2[2], v3 * gb2[3]);
    float s1 = (v0 + v1) + (v2 + v3), s2 = (v0 * v0 + v1 * v1) + (v2 * v2 + v3 * v3);
    s1 += __shfl_xor(s1, 16); s2 += __shfl_xor(s2, 16);
    s1 += __shfl_xor(s1, 32); s2 += __shfl_xor(s2, 32);
    if (g == 0) { red[w * 64 + t * 16 + c] = s1; red[w * 64 + 32 + t * 16 + c] = s2; }
  }
  bst16<AUX_K3>(rs3, lane * 16, (unsigned)X1_OFF + (((rb >> 1) * 36 + T) * 2 + (rb & 1)) * 1024, xo);
  swap16(og[0][0], og[1][0]); swap16(og[0][1], og[1][1]);
  bst16<AUX_K3G>(rs3, (g & 1) * 1024 + ((2 * (T & 1) + (g >> 1)) * 16 + c) * 16,
                 (unsigned)X1G_OFF + (rb >> 1) * 73728 + ((T >> 1) * 4 + (rb & 1) * 2) * 1024, u32x4{og[0][0], og[0][1], og[1][0], og[1][1]});
  asm volatile("s_waitcnt lgkmcnt(0)\n\ts_barrier" ::: "memory");
  if (tid < 64) {
    float sm = 0.f;
#pragma unroll
    for (int i = 0; i < 9; ++i) sm += red[i * 64 + tid];
    ((float*)(ws + STAT_OFF))[(rb * 4 + cg) * 64 + tid] = sm;
  }
  TS(3, 2); TSEND(3);
}

#define K4_X 0
#define K4_WIN 8
struct Silu { float z[4], e[4]; unsigned p[2]; };
constexpr int SOPS = 27;
#define PIN(x) asm volatile("" : "+v"(x))
template <int k>
DI void silu_op(Silu& q, const f32x4& v, const f32x4& cs, const f32x4& bs, float rs, float nm, LAS char* dst) {
  if constexpr (k < 4) { q.z[k] = __builtin_fmaf(cs[k], nm, bs[k]); PIN(q.z[k]); }
  else if constexpr (k < 8) { q.z[k - 4] = __builtin_fmaf(v[k - 4], rs, q.z[k - 4]); PIN(q.z[k - 4]); }
  else if constexpr (k < 12) { q.e[k - 8] = __builtin_amdgcn_exp2f(q.z[k - 8]); PIN(q.e[k - 8]); }
  else if constexpr (k < 16) { q.e[k - 12] = 1.f + q.e[k - 12]; PIN(q.e[k - 12]); }
  else if constexpr (k < 20) { q.e[k - 16] = __builtin_amdgcn_rcpf(q.e[k - 16]); PIN(q.e[k - 16]); }
  else if constexpr (k < 24) { q.e[k - 20] = q.z[k - 20] * q.e[k - 20]; PIN(q.e[k - 20]); }
  else if constexpr (k < 26) { q.p[k - 24] = cvtpk(q.e[2 * (k - 24)], q.e[2 * (k - 24) + 1]); PIN(q.p[k - 24]); }
  else *(LAS u32x2*)dst = u32x2{q.p[0], q.p[1]};
}
template <int NT>
DI void mlp_body(char* __restrict__ ws, LAS char* hb, LAS char* h1, LAS float* st, LAS float* csb, int w, int lane, int fr, int rb,
                 f32x4 sv0, f32x4 sv1 PROFARG) {
  constexpr int NB = NT - 2, NF = NT * KS, WIN = K4_WIN;
  const int T0 = NT == 5 ? 5 * w : 20 + 4 * (w - 4);
  const int TA = 2 * w, TB = NT == 5 ? 16 + 3 * w : 28 + 2 * (w - 4);
  const unsigned wbase = (unsigned)(fr * 648 + (NT == 5 ? w * 90 : 360 + (w - 4) * 72)) * 1024u;
  const rsrc_t rs_ = ws_rsrc(ws);
  const unsigned vo = lane * 16;
  const unsigned o1 = (unsigned)W1T_OFF + wbase, o2 = (unsigned)W2T_OFF + wbase;
#define K4_EXP 0
#define WLOAD(i) bld16(rs_, vo, (i) < NF ? o1 + (i) * 1024u : o2 + ((i) - NF) * 1024u)
  u32x4 a[WIN];
#pragma unroll
  for (int i = 0; i < WIN; ++i) a[i] = WLOAD(i);
  SBAR();
  constexpr float NL2E = -1.4426950408889634f;
  {
    const int tid = w * 64 + lane;
    if (tid < 64) {
      const float s1 = (sv0[0] + sv0[1]) + (sv0[2] + sv0[3]), s2 = (sv1[0] + sv1[1]) + (sv1[2] + sv1[3]);
      const float mean = s1 * (1.f / D);
      const float var = __builtin_fmaxf(s2 * (1.f / D) - mean * mean, 0.f);
      const float rstd = __builtin_amdgcn_rsqf(var + 1e-5f);
      st[2 * tid] = rstd * NL2E; st[2 * tid + 1] = -mean * rstd;
    } else if (tid < 352) {
      *(LAS f32x4*)(csb + 4 * (tid - 64)) = sv0;
    }
  }
  asm volatile("s_waitcnt vmcnt(%0) lgkmcnt(0)\n\ts_barrier" :: "i"(WIN) : "memory");
  TS(4, 2);
  unsigned bp0 = (unsigned)(size_t)hb + lane * 16, bp1 = bp0 + 40960, hp0 = (unsigned)(size_t)h1 + lane * 16, hp1 = hp0 + 40960;
  asm volatile("" : "+v"(bp0), "+v"(bp1), "+v"(hp0), "+v"(hp1));
#define XFRAG(f) lds128((const LAS char*)(size_t)((f) < 40 ? bp0 + (f) * 1024 : bp1 + ((f) - 40) * 1024))
#define HFRAG(f) lds128((const LAS char*)(size_t)((f) < 40 ? hp0 + (f) * 1024 : hp1 + ((f) - 40) * 1024))
  const int g = lane >> 4, c = lane & 15;
  f32x4 accA[2][4], accB[NB][4], acc[NT][4];
  u32x4 bf[2][4];
  const f32x4 z4 = {0.f, 0.f, 0.f, 0.f};
#pragma unroll
  for (int t = 0; t < 4; ++t) bf[0][t] = XFRAG(t);
#pragma unroll
  for (int s = 0; s < KS; ++s) {
#pragma unroll
    for (int t = 0; t < 4; ++t) bf[(s + 1) & 1][t] = XFRAG(((s + 1) % KS) * 4 + t);
    SBAR();
#pragma unroll
    for (int j = 0; j < 2; ++j) {
      const int i = s * 2 + j;
#pragma unroll
      for (int t = 0; t < 4; ++t) accA[j][t] = mfma16(a[i % WIN], bf[s & 1][t], s == 0 ? z4 : accA[j][t]);
      SBAR();
      a[i % WIN] = WLOAD(i + WIN);
      SBAR();
    }
  }
  TS(4, 3);
  float rs[4], nm[4];
#pragma unroll
  for (int t = 0; t < 4; ++t) { rs[t] = st[2 * (t * 16 + c)]; nm[t] = st[2 * (t * 16 + c) + 1]; }
  const LAS float* csg = csb + 4 * g;
  f32x4 cs[2], bs[2];
  cs[0] = *(const LAS f32x4*)(csg + TA * 16); bs[0] = *(const LAS f32x4*)(csg + 576 + TA * 16);
  cs[1] = *(const LAS f32x4*)(csg + TA * 16 + 16); bs[1] = *(const LAS f32x4*)(csg + 576 + TA * 16 + 16);
  Silu sq;
  sfor<0, KS>(LAM(s_) {
    constexpr int s = s_.v, M = 16 * NB * 4, TOT = 8 * SOPS;
    if constexpr (s + 1 < KS) {
#pragma unroll
      for (int t = 0; t < 4; ++t) bf[(s + 1) & 1][t] = XFRAG((s + 1) * 4 + t);
    }
    SBAR();
    sfor<0, NB>(LAM(j_) {
      constexpr int j = j_.v, i = 36 + s * NB + j;
      sfor<0, 4>(LAM(t_) {
        constexpr int t = t_.v, m = (s * NB + j) * 4 + t;
        accB[j][t] = mfma16(a[i % WIN], bf[s & 1][t], s == 0 ? z4 : accB[j][t]);
        PIN(accB[j][t]);
        if constexpr (m < M) {
          sfor<m * TOT / M, (m + 1) * TOT / M>(LAM(o_) {
            constexpr int o = o_.v, q = o / SOPS, jj = q >> 2, tt = q & 3;
            silu_op<o % SOPS>(sq, accA[jj][tt], cs[jj], bs[jj], rs[tt], nm[tt], h1 + (w * 4 + tt) * 1024 + lane * 16 + jj * 8);
          });
        }
        SBAR();
      });
      a[i % WIN] = WLOAD(i + WIN);
      SBAR();
    });
  });
  TS(4, 4);
  cs[0] = *(const LAS f32x4*)(csg + TB * 16); bs[0] = *(const LAS f32x4*)(csg + 576 + TB * 16);
  cs[1] = *(const LAS f32x4*)(csg + TB * 16 + 16); bs[1] = *(const LAS f32x4*)(csg + 576 + TB * 16 + 16);
  asm volatile("s_waitcnt lgkmcnt(0)\n\ts_barrier" ::: "memory");
  TS(4, 5);
#pragma unroll
  for (int t = 0; t < 4; ++t) bf[0][t] = HFRAG(t);
  sfor<0, 8>(LAM(s_) {
    constexpr int s = s_.v, M = 8 * NT * 4, TOT = NB * 4 * SOPS;
    if constexpr (s + 1 < 8) {
#pragma unroll
      for (int t = 0; t < 4; ++t) bf[(s + 1) & 1][t] = HFRAG((s + 1) * 4 + t);
    }
    SBAR();
    sfor<0, NT>(LAM(j_) {
      constexpr int j = j_.v, i = NF + s * NT + j;
      sfor<0, 4>(LAM(t_) {
        constexpr int t = t_.v, m = (s * NT + j) * 4 + t;
        acc[j][t] = mfma16(a[i % WIN], bf[s & 1][t], s == 0 ? z4 : acc[j][t]);
        PIN(acc[j][t]);
        sfor<m * TOT / M, (m + 1) * TOT / M>(LAM(o_) {
          constexpr int o = o_.v, q = o / SOPS, jj = q >> 2, tt = q & 3;
          const int T = TB + jj;
          if constexpr (NB == 3 && o == 4 * SOPS + 8) {
            cs[0] = *(const LAS f32x4*)(csg + TB * 16 + 32); bs[0] = *(const LAS f32x4*)(csg + 576 + TB * 16 + 32);
          }
          silu_op<o % SOPS>(sq, accB[jj][tt], cs[jj & 1], bs[jj & 1], rs[tt], nm[tt], h1 + ((T >> 1) * 4 + tt) * 1024 + lane * 16 + (T & 1) * 8);
        });
        SBAR();
      });
      a[i % WIN] = WLOAD(i + WIN);
      SBAR();
    });
  });
  TS(4, 6);
  asm volatile("s_waitcnt lgkmcnt(0)\n\ts_barrier" ::: "memory");
  TS(4, 7);
#pragma unroll
  for (int t = 0; t < 4; ++t) bf[0][t] = HFRAG(32 + t);
#pragma unroll
  for (int s = 8; s < KS; ++s) {
    if (s + 1 < KS) {
#pragma unroll
      for (int t = 0; t < 4; ++t) bf[(s + 1) & 1][t] = HFRAG((s + 1) * 4 + t);
    }
    SBAR();
#pragma unroll
    for (int j = 0; j < NT; ++j) {
      const int i = NF + s * NT + j;
#pragma unroll
      for (int t = 0; t < 4; ++t) acc[j][t] = mfma16(a[i % WIN], bf[s & 1][t], acc[j][t]);
      SBAR();
      if (i + WIN < 2 * NF) a[i % WIN] = WLOAD(i + WIN);
      SBAR();
    }
  }
  TS(4, 8);
#pragma unroll
  for (int j = 0; j < NT; ++j) {
    const int T = T0 + j;
#pragma unroll
    for (int tp = 0; tp < 2; ++tp) {
      const f32x4 v0 = acc[j][2 * tp], v1 = acc[j][2 * tp + 1];
      u32x4 o = {cvtpk(v0[0], v0[1]), cvtpk(v0[2], v0[3]), cvtpk(v1[0], v1[1]), cvtpk(v1[2], v1[3])};
      bst16<AUX_PART>(rs_, vo, (unsigned)(PART_OFF + ((((size_t)fr * 32 + rb) * 36 + T) * 2 + tp) * 1024), o);
    }
  }
  TS(4, 9); TSEND(4);
#undef WLOAD
#undef XFRAG
#undef HFRAG
}
#define K4_MAP 2
__global__ void __launch_bounds__(512, 2) k4_mlp(char* __restrict__ ws) {
  __shared__ __attribute__((aligned(16))) char lds[2 * 73728 + 512 + 4608];
  const int tid = threadIdx.x, lane = tid & 63, w = __builtin_amdgcn_readfirstlane(tid >> 6);
  const int xcd = blockIdx.x & 7, idx = blockIdx.x >> 3;
  const int rb = (xcd >> 1) * 8 + (idx & 7), fr = (xcd & 1) * 4 + (idx >> 3);
  LAS char* hb = (LAS char*)lds;
  LAS char* h1 = hb + 73728;
  LAS float* st = (LAS float*)(hb + 2 * 73728);
  LAS float* csb = st + 128;
  TSDECL; TS(4, 0);
  f32x4 sv0 = {0.f, 0.f, 0.f, 0.f}, sv1 = sv0;
  if (w == 0) {
    const float* sp = (const float*)(ws + STAT_OFF) + (size_t)(rb * 2 + (tid >> 5)) * 256 + (tid & 31);
    sv0 = f32x4{sp[0], sp[64], sp[128], sp[192]};
    sv1 = f32x4{sp[32], sp[96], sp[160], sp[224]};
  } else {
    const int i = tid < 352 ? tid - 64 : 0;
    sv0 = *(const f32x4*)(ws + CSBF_OFF + ((size_t)(i >= 144) * F + fr * 576 + 4 * (i >= 144 ? i - 144 : i)) * 4);
  }
  const char* src = ws + X1G_OFF + (size_t)rb * 73728 + w * 9216 + lane * 16;
  const unsigned lb = (unsigned)(size_t)hb + w * 9216;
#pragma unroll
  for (int i = 0; i < 9; ++i) glds16(src + i * 1024, lb + i * 1024);
  if (w < 4) mlp_body<5>(ws, hb, h1, st, csb, w, lane, fr, rb, sv0, sv1 PROFPASS);
  else mlp_body<4>(ws, hb, h1, st, csb, w, lane, fr, rb, sv0, sv1 PROFPASS);
}

__global__ void __launch_bounds__(576) k5_out(char* __restrict__ ws, u16* __restrict__ out) {
  const int tid = threadIdx.x, lane = tid & 63, w = tid >> 6;
  TSDECL; TS(5, 0);
  const int rb = blockIdx.x >> 3, sub = blockIdx.x & 7;
  const int unit = sub * 9 + w, T = unit >> 1, tp = unit & 1;
  const int g = lane >> 4, c = lane & 15;
  u32x4 p[8];
#pragma unroll
  for (int f = 0; f < 8; ++f) p[f] = *(const u32x4*)(ws + PART_OFF + ((((size_t)f * 32 + rb) * 36 + T) * 2 + tp) * 1024 + lane * 16);
  const int col = T * 16 + 4 * g;
  const u32x4 xr = *(const u32x4*)(ws + X1_OFF + (((size_t)rb * 36 + T) * 2 + tp) * 1024 + lane * 16);
  const f32x4 gb = *(const f32x4*)(ws + MODF_OFF + (5 * D + col) * 4) * -0.6931471805599453f;
#pragma unroll
  for (int t = 0; t < 2; ++t) {
    float a[4] = {0.f, 0.f, 0.f, 0.f};
#pragma unroll
    for (int f = 0; f < 8; ++f) {
      a[0] += bflo(p[f][2 * t]); a[1] += bfhi(p[f][2 * t]);
      a[2] += bflo(p[f][2 * t + 1]); a[3] += bfhi(p[f][2 * t + 1]);
    }
    u32x2 o = {cvtpk(bflo(xr[2 * t]) + gb[0] * a[0], bfhi(xr[2 * t]) + gb[1] * a[1]),
               cvtpk(bflo(xr[2 * t + 1]) + gb[2] * a[2], bfhi(xr[2 * t + 1]) + gb[3] * a[3])};
    *(u32x2*)(out + (size_t)(rb * 64 + tp * 32 + t * 16 + c) * D + col) = o;
  }
  TS(5, 1); TSEND(5);
}

extern "C" void kernel_launch(void* const* d_in, const int* in_sizes, int n_in, void* d_out, int out_size, void* d_ws,
                              size_t ws_size, hipStream_t stream) {
  const u16* x = (const u16*)d_in[0];
  const u16* wqkv = (const u16*)d_in[1];
  const u16* wo = (const u16*)d_in[2];
  const u16* w1 = (const u16*)d_in[4];
  const u16* w2 = (const u16*)d_in[5];
  const u16* c = (const u16*)d_in[6];
  const u16* wmod = (const u16*)d_in[7];
  char* ws = (char*)d_ws;
  k0_prep<<<K0_MOD + K0_LN + K0_QKV_BLKS, 256, 0, stream>>>(c, wmod, wqkv, x, ws);
  k1_qkv<<<256, 512, 0, stream>>>(ws);
  k2_attn<<<256, 768, 0, stream>>>(w1, w2, wo, ws);
  k3_wo<<<256, 576, 0, stream>>>(x, ws);
  k4_mlp<<<256, 512, 0, stream>>>(ws);
  k5_out<<<256, 576, 0, stream>>>(ws, (u16*)d_out);
}
```

```cpp
#include <hip/hip_runtime.h>
#include <hip/hip_bf16.h>
#include <stdint.h>

typedef unsigned short u16;
typedef short bf16x8 __attribute__((ext_vector_type(8)));
typedef short s16x4 __attribute__((ext_vector_type(4)));
typedef float f32x4 __attribute__((ext_vector_type(4)));
typedef float f32x16 __attribute__((ext_vector_type(16)));
typedef unsigned u32x4 __attribute__((ext_vector_type(4)));
typedef unsigned u32x2 __attribute__((ext_vector_type(2)));
#define LAS __attribute__((address_space(3)))
#define GAS __attribute__((address_space(1)))
#define DI __device__ __forceinline__

constexpr int S = 2048, D = 576, NH = 32, DH = 18, F = 4608;
constexpr int KS = 18;

constexpr size_t MODP_OFF = 0;
constexpr size_t WQKVT_OFF = 0x10000;
constexpr size_t WOT_OFF = WQKVT_OFF + 8 * 14 * 18 * 1024;
constexpr size_t W1T_OFF = WOT_OFF + 36 * 18 * 1024;
constexpr size_t W2T_OFF = W1T_OFF + 8 * 648 * 1024;
constexpr size_t QLO_OFF = W2T_OFF + 8 * 648 * 1024;
constexpr size_t QHI_OFF = QLO_OFF + 32 * 64 * 1024;
constexpr int KVT = 2432;
constexpr int KVH = 64 * KVT;
constexpr size_t KV_OFF = QHI_OFF + 32 * 2048 * 4;
constexpr size_t OF_OFF = KV_OFF + (size_t)32 * KVH;
constexpr size_t X1_OFF = OF_OFF + (size_t)S * D * 2;
constexpr size_t X1G_OFF = X1_OFF + (size_t)S * D * 2;
constexpr size_t NF_OFF = X1G_OFF + (size_t)S * D * 2;
constexpr size_t STAT_OFF = NF_OFF + (size_t)S * D * 2;
constexpr size_t CSB_OFF = STAT_OFF + 64 * 4 * 64 * 4;
constexpr size_t CSBF_OFF = CSB_OFF + 6 * 2 * 4608 * 4;
constexpr size_t MODF_OFF = CSBF_OFF + 2 * 4608 * 4;
constexpr size_t CNT_OFF = MODF_OFF + 6 * D * 4;
constexpr size_t PART_OFF = CSB_OFF + 0x50000;
constexpr size_t WS_END = PART_OFF + (size_t)8 * S * D * 2;

#define TSDECL
#define TS(kid, i)
#define TSEND(kid)
#define PROFARG
#define PROFPASS
template <int I> struct IC { static constexpr int v = I; };
template <int B, int E, class Fn> __device__ __forceinline__ void sfor(Fn&& f) {
  if constexpr (B < E) { f(IC<B>{}); sfor<B + 1, E>(f); }
}
#define LAM(x) [&](auto x) __attribute__((always_inline))
DI float bf2f(unsigned v) { return __builtin_bit_cast(float, v << 16); }
DI float bflo(unsigned v) { return __builtin_bit_cast(float, v << 16); }
DI float bfhi(unsigned v) { return __builtin_bit_cast(float, v & 0xffff0000u); }
typedef __bf16 bf16x2_t __attribute__((ext_vector_type(2)));
typedef float f32x2 __attribute__((ext_vector_type(2)));
DI unsigned cvtpk(float lo, float hi) {
  return __builtin_bit_cast(unsigned, __builtin_convertvector(f32x2{lo, hi}, bf16x2_t));
}
DI float rbf(float x) { return bflo(cvtpk(x, x)); }
DI f32x4 mfma16(u32x4 a, u32x4 b, f32x4 c) {
  return __builtin_amdgcn_mfma_f32_16x16x32_bf16(__builtin_bit_cast(bf16x8, a), __builtin_bit_cast(bf16x8, b), c, 0, 0, 0);
}
DI f32x16 mfma32(u32x4 a, u32x4 b, f32x16 c) {
  return __builtin_amdgcn_mfma_f32_32x32x16_bf16(__builtin_bit_cast(bf16x8, a), __builtin_bit_cast(bf16x8, b), c, 0, 0, 0);
}
DI void glds16(const void* g, unsigned lds_base) {
  unsigned sv;
  asm volatile("s_mov_b32 %0, m0\n\ts_mov_b32 m0, %2\n\ts_nop 0\n\tglobal_load_lds_dwordx4 %1, off\n\ts_mov_b32 m0, %0"
               : "=&s"(sv) : "v"(g), "s"(lds_base) : "memory");
}
typedef __amdgpu_buffer_rsrc_t rsrc_t;
DI rsrc_t ws_rsrc(const char* ws) { return __builtin_amdgcn_make_buffer_rsrc((void*)ws, (short)0, (int)WS_END, 0x00020000); }
template <int AUX> DI void bst16(rsrc_t r, unsigned voff, unsigned soff, u32x4 v) { __builtin_amdgcn_raw_buffer_store_b128(v, r, voff + soff, 0, AUX); }
template <int AUX> DI void bst8(rsrc_t r, unsigned voff, unsigned soff, u32x2 v) { __builtin_amdgcn_raw_buffer_store_b64(v, r, voff, soff, AUX); }
template <int AUX> DI void bst4(rsrc_t r, unsigned voff, unsigned soff, unsigned v) { __builtin_amdgcn_raw_buffer_store_b32(v, r, voff, soff, AUX); }
DI u32x4 bld16(rsrc_t r, unsigned voff, unsigned soff) { return __builtin_amdgcn_raw_buffer_load_b128(r, voff, soff, 0); }
#define AUX_RT 16
#define AUX_K0L 16
#define AUX_K0G 16
#define AUX_O 16
#define AUX_PART 16
#define WAIT_VM(n) asm volatile("s_waitcnt vmcnt(" #n ")" ::: "memory")
#define SBAR() __builtin_amdgcn_sched_barrier(0)
DI float silu_f(float x) { return x * __builtin_amdgcn_rcpf(1.f + __builtin_amdgcn_exp2f(-1.4426950408889634f * x)); }
typedef short v4i16_t __attribute__((ext_vector_type(4)));
DI u32x2 tr_read(const LAS char* p) {
  return __builtin_bit_cast(u32x2, __builtin_amdgcn_ds_read_tr16_b64_v4i16((LAS v4i16_t*)p));
}
template <int CTRL> DI unsigned dppu(unsigned v) { return __builtin_amdgcn_update_dpp(0u, v, CTRL, 0xF, 0xF, true); }
DI unsigned dpp_xor1(unsigned v) { return dppu<0xB1>(v); }
template <int CTRL> DI float dppf(float v) { return __builtin_bit_cast(float, dppu<CTRL>(__builtin_bit_cast(unsigned, v))); }
DI float sum16(float v) { v += dppf<0xB1>(v); v += dppf<0x4E>(v); v += dppf<0x141>(v); v += dppf<0x140>(v); return v; }
DI u32x4 lds128(const LAS char* p) { return *(const LAS u32x4*)p; }

DI int frag_index(int T, int s) {
  return T < 20 ? (T / 5) * 90 + s * 5 + (T % 5) : 360 + ((T - 20) >> 2) * 72 + s * 4 + ((T - 20) & 3);
}

DI int frag_index1(int T, int s) {
  if (T < 16) { const int w = T >> 1; return (w < 4 ? 90 * w : 360 + 72 * (w - 4)) + 2 * s + (T & 1); }
  if (T < 28) { const int w = (T - 16) / 3; return 90 * w + 36 + 3 * s + (T - 16) % 3; }
  const int w = 4 + ((T - 28) >> 1);
  return 360 + 72 * (w - 4) + 36 + 2 * s + (T & 1);
}

constexpr int K0_MOD = 216, K0_LN = 128, K0_QKV_BLKS = 144;
__global__ void __launch_bounds__(256) k0_prep(const u16* __restrict__ c, const u16* __restrict__ wmod, const u16* __restrict__ wqkv,
                                               const u16* __restrict__ x, char* __restrict__ ws) {
  __shared__ __attribute__((aligned(16))) char lds[18432];
  const int tid = threadIdx.x, lane = tid & 63, w = __builtin_amdgcn_readfirstlane(tid >> 6);
  int b = blockIdx.x;
  TSDECL; TS(0, 0);
  if (b < K0_MOD) {
    const int cc = b % 54, ks = b / 54;
    const int ch = tid & 7, rg = tid >> 3;
    float acc[8];
#pragma unroll
    for (int j = 0; j < 8; ++j) acc[j] = 0.f;
    u32x4 wv[5];
    float sv[5];
    unsigned cr[5];
#pragma unroll
    for (int i = 0; i < 5; ++i) {
      const int r = rg + 32 * i, k = ks * 144 + (r < 144 ? r : 0);
      cr[i] = c[k];
      wv[i] = *(const u32x4*)(wmod + (size_t)k * 3456 + cc * 64 + ch * 8);
    }
    SBAR();
#pragma unroll
    for (int i = 0; i < 5; ++i) sv[i] = rbf(silu_f(bf2f(cr[i]))) * (rg + 32 * i < 144 ? 1.f : 0.f);
#pragma unroll
    for (int i = 0; i < 5; ++i)
#pragma unroll
      for (int j = 0; j < 4; ++j) {
        acc[2 * j] += sv[i] * bflo(wv[i][j]);
        acc[2 * j + 1] += sv[i] * bfhi(wv[i][j]);
      }
#pragma unroll
    for (int j = 0; j < 8; ++j) {
      acc[j] += __shfl_xor(acc[j], 8);
      acc[j] += __shfl_xor(acc[j], 16);
      acc[j] += __shfl_xor(acc[j], 32);
    }
    float* red = (float*)lds;
    if (lane < 8)
#pragma unroll
      for (int j = 0; j < 8; ++j) red[w * 64 + lane * 8 + j] = acc[j];
    __syncthreads();
    if (tid < 64) ((float*)(ws + MODP_OFF))[ks * 3456 + cc * 64 + tid] = red[tid] + red[64 + tid] + red[128 + tid] + red[192 + tid];
    TS(0, 1); TSEND(0);
    return;
  }
  b -= K0_MOD;
  if (b < K0_LN) {
    const int tok = b * 16 + w * 4 + (lane >> 4), p = lane & 15;
    const u16* xp = x + (size_t)tok * D + 4 * p;
    u32x2 xv[9];
#pragma unroll
    for (int i = 0; i < 9; ++i) xv[i] = *(const u32x2*)(xp + 64 * i);
    float sm = 0.f;
#pragma unroll
    for (int i = 0; i < 9; ++i) sm += (bflo(xv[i][0]) + bfhi(xv[i][0])) + (bflo(xv[i][1]) + bfhi(xv[i][1]));
    const float mean = sum16(sm) * (1.f / D);
    float q = 0.f;
#pragma unroll
    for (int i = 0; i < 9; ++i)
#pragma unroll
      for (int j = 0; j < 2; ++j) { float a0 = bflo(xv[i][j]) - mean, a1 = bfhi(xv[i][j]) - mean; q += a0 * a0; q += a1 * a1; }
    const float rstd = __builtin_amdgcn_rsqf(sum16(q) * (1.f / D) + 1e-5f);
    LAS char* dst = (LAS char*)lds + (p >> 3) * 1024 + ((p >> 1) & 3) * 256 + (tok & 15) * 16 + (p & 1) * 8;
#pragma unroll
    for (int i = 0; i < 9; ++i) {
      u32x2 o = {cvtpk((bflo(xv[i][0]) - mean) * rstd, (bfhi(xv[i][0]) - mean) * rstd),
                 cvtpk((bflo(xv[i][1]) - mean) * rstd, (bfhi(xv[i][1]) - mean) * rstd)};
      *(LAS u32x2*)(dst + i * 2048) = o;
    }
    __syncthreads();
    const rsrc_t rs0 = ws_rsrc(ws);
#pragma unroll
    for (int i = 0; i < 5; ++i) {
      const int s = w + 4 * i;
      if (s < 18) bst16<AUX_K0L>(rs0, lane * 16, (unsigned)NF_OFF + (b >> 2) * 73728 + s * 4096 + (b & 3) * 1024, lds128((const LAS char*)lds + s * 1024 + lane * 16));
    }
    TS(0, 1); TSEND(0);
    return;
  }
  b -= K0_LN;
  {
    const int s = b % 18, hq = b / 18;
    constexpr int PITCH = 448;
    u32x4 raw[4];
#pragma unroll
    for (int i = 0; i < 4; ++i) {
      const int e = min(tid + 256 * i, 863), row = e / 27, pc = e % 27;
      raw[i] = *(const u32x4*)(wqkv + (size_t)(32 * s + row) * 1728 + (pc / 9) * 576 + hq * 72 + (pc % 9) * 8);
    }
#pragma unroll
    for (int i = 0; i < 4; ++i) {
      const int e = min(tid + 256 * i, 863), row = e / 27, pc = e % 27;
      *(LAS u32x4*)((LAS char*)lds + row * PITCH + pc * 16) = raw[i];
    }
    __syncthreads();
    const int g = lane >> 4, r = lane & 15;
    const rsrc_t rs0 = ws_rsrc(ws);
#pragma unroll
    for (int i = 0; i < 4; ++i) {
      const int tile = w + 4 * i;
      if (tile < 14) {
        int col; bool ok = true;
        if (tile < 12) col = (tile >> 2) * 72 + (tile & 3) * 18 + r;
        else if (tile == 12) col = (r >> 3) * 72 + ((r >> 1) & 3) * 18 + 16 + (r & 1);
        else { ok = r < 8; col = 144 + ((r >> 1) & 3) * 18 + 16 + (r & 1); }
        const LAS u16* p = (const LAS u16*)((LAS char*)lds + 8 * g * PITCH) + col;
        u32x4 o;
#pragma unroll
        for (int j = 0; j < 4; ++j) o[j] = ok ? ((unsigned)p[(2 * j) * (PITCH / 2)] | ((unsigned)p[(2 * j + 1) * (PITCH / 2)] << 16)) : 0u;
        bst16<AUX_K0G>(rs0, lane * 16, (unsigned)WQKVT_OFF + ((hq * 14 + tile) * 18 + s) * 1024, o);
      }
    }
    TS(0, 1); TSEND(0);
  }
}

DI f32x4 mod4(const float* __restrict__ modp, int col) {
  const f32x4 a = *(const f32x4*)(modp + col), b = *(const f32x4*)(modp + 3456 + col), c = *(const f32x4*)(modp + 2 * 3456 + col),
              d = *(const f32x4*)(modp + 3 * 3456 + col);
  return f32x4{rbf(a[0] + b[0] + c[0] + d[0]), rbf(a[1] + b[1] + c[1] + d[1]), rbf(a[2] + b[2] + c[2] + d[2]), rbf(a[3] + b[3] + c[3] + d[3])};
}
DI f32x4 gain4(f32x4 v) { return f32x4{rbf(1.f + v[0]), rbf(1.f + v[1]), rbf(1.f + v[2]), rbf(1.f + v[3])}; }

constexpr float QSCALE = 0.23570226039551584f * 1.4426950408889634f;
DI u32x4 modulate8(u32x4 v, const f32x4& g0, const f32x4& g1, const f32x4& s0, const f32x4& s1) {
  return u32x4{cvtpk(bflo(v[0]) * g0[0] + s0[0], bfhi(v[0]) * g0[1] + s0[1]), cvtpk(bflo(v[1]) * g0[2] + s0[2], bfhi(v[1]) * g0[3] + s0[3]),
               cvtpk(bflo(v[2]) * g1[0] + s1[0], bfhi(v[2]) * g1[1] + s1[1]), cvtpk(bflo(v[3]) * g1[2] + s1[2], bfhi(v[3]) * g1[3] + s1[3])};
}
#define AUX_K1 16
#define RAWBAR() asm volatile("s_barrier" ::: "memory")
#define LDSBAR() asm volatile("s_waitcnt lgkmcnt(0)\n\ts_barrier" ::: "memory")
DI void swap16(unsigned& a, unsigned& b) { const auto r = __builtin_amdgcn_permlane16_swap(a, b, false, false); a = r[0]; b = r[1]; }
DI void swap32(unsigned& a, unsigned& b) { const auto r = __builtin_amdgcn_permlane32_swap(a, b, false, false); a = r[0]; b = r[1]; }
constexpr int K1_WIN = 12;
constexpr unsigned OOB = 0x40000000u;
template <bool N0, bool N1>
DI void qkv_gemm(rsrc_t rs, unsigned vo, unsigned so, u32x4 (&a)[K1_WIN], const LAS char* b0, f32x4 (&acc)[2][4]) {
  constexpr int WIN = K1_WIN;
  const f32x4 z4 = {0.f, 0.f, 0.f, 0.f};
#pragma unroll
  for (int j = 0; j < 2; ++j)
#pragma unroll
    for (int t = 0; t < 4; ++t) acc[j][t] = z4;
  u32x4 bf[2][4];
#pragma unroll
  for (int t = 0; t < 4; ++t) bf[0][t] = lds128(b0 + t * 1024);
#pragma unroll 1
  for (int it = 0; it < 3; ++it) {
    const unsigned vn = it < 2 ? vo : OOB;
#pragma unroll
    for (int u = 0; u < 6; ++u) {
      const LAS char* bn = b0 + (u + 1) * 4096;
#pragma unroll
      for (int t = 0; t < 4; ++t) bf[(u + 1) & 1][t] = lds128(bn + t * 1024);
      SBAR();
#pragma unroll
      for (int j = 0; j < 2; ++j) {
#pragma unroll
        for (int t = 0; t < 4; ++t)
          acc[j][t] = (j == 0 ? N0 : N1) ? mfma16(bf[u & 1][t], a[2 * u + j], acc[j][t]) : mfma16(a[2 * u + j], bf[u & 1][t], acc[j][t]);
        SBAR();
        a[2 * u + j] = bld16(rs, vn, so + (j * 18 + 6 + u) * 1024);
        SBAR();
      }
    }
    b0 += 6 * 4096; so += 6 * 1024;
  }
}
template <bool GEMM, bool N0, bool N1>
DI void qkv_body(rsrc_t rs, unsigned vo, unsigned so, const u32x4 (&xv)[9], LAS char* hb, const LAS float* modv, int w, int lane,
                 f32x4 (&acc)[2][4] PROFARG) {
  u32x4 a[K1_WIN];
  const LAS float* gp = modv + (lane >> 4) * 8;
  f32x4 gs[3][4];
#pragma unroll
  for (int k = 0; k < 3; ++k) {
    const LAS float* p = gp + (k < 2 ? 2 * w + k : 16 + (w >> 2)) * 32;
    gs[k][0] = *(const LAS f32x4*)p; gs[k][1] = *(const LAS f32x4*)(p + 4);
    gs[k][2] = *(const LAS f32x4*)(p + 576); gs[k][3] = *(const LAS f32x4*)(p + 580);
  }
  SBAR();
  sfor<0, 9>(LAM(i_) {
    constexpr int i = i_.v, l0 = i < 3 ? 2 * i : i + 3, l1 = i < 3 ? 2 * i + 2 : i + 4;
    if constexpr (GEMM) sfor<l0, l1>(LAM(l_) { constexpr int l = l_.v; a[l] = bld16(rs, vo, so + ((l & 1) * 18 + (l >> 1)) * 1024); });
    SBAR();
    const u32x4 m = modulate8(xv[i], gs[i >> 2][0], gs[i >> 2][1], gs[i >> 2][2], gs[i >> 2][3]);
    *(LAS u32x4*)(hb + (i < 8 ? 8 * w + i : 64 + w) * 1024 + lane * 16) = m;
    SBAR();
  });
  TS(1, 2);
  LDSBAR();
  TS(1, 3);
  if constexpr (GEMM) qkv_gemm<N0, N1>(rs, vo, so, a, hb + lane * 16, acc);
}
__global__ void __launch_bounds__(512, 2) k1_qkv(char* __restrict__ ws) {
  __shared__ __attribute__((aligned(16))) char lds[73728 + 4608];
  const int tid = threadIdx.x, lane = tid & 63, w = __builtin_amdgcn_readfirstlane(tid >> 6);
  const int xcd = blockIdx.x & 7, idx = blockIdx.x >> 3;
  const int rb = (xcd >> 1) * 8 + (idx & 7), hq = (xcd & 1) * 4 + (idx >> 3);
  LAS char* hb = (LAS char*)lds;
  LAS float* modv = (LAS float*)(hb + 73728);
  const rsrc_t rs = ws_rsrc(ws);
  const unsigned vo = lane * 16;
  TSDECL; TS(1, 0);
  const bool domod = tid < 288;
  const unsigned mo = domod ? (unsigned)MODP_OFF + (tid < 144 ? D + 4 * tid : 4 * (tid - 144)) * 4 : OOB;
  u32x4 mp[4];
#pragma unroll
  for (int i = 0; i < 4; ++i) mp[i] = bld16(rs, mo, i * 3456 * 4);
  TS(1, 6);
  SBAR(); RAWBAR(); SBAR();
  TS(1, 7);
  u32x4 xv[9];
  {
    const unsigned no = (unsigned)NF_OFF + rb * 73728;
#pragma unroll
    for (int i = 0; i < 8; ++i) xv[i] = bld16(rs, vo, no + (8 * w + i) * 1024);
    xv[8] = bld16(rs, vo, no + (64 + w) * 1024);
  }
  SBAR();
  TS(1, 8);
  {
    const f32x4 ms = __builtin_bit_cast(f32x4, mp[0]) + __builtin_bit_cast(f32x4, mp[1]) + __builtin_bit_cast(f32x4, mp[2]) + __builtin_bit_cast(f32x4, mp[3]);
    const f32x4 mv = {rbf(ms[0]), rbf(ms[1]), rbf(ms[2]), rbf(ms[3])};
    *(LAS f32x4*)(domod ? (LAS char*)(modv + 4 * tid) : hb + 16 * tid) = tid < 144 ? gain4(mv) : mv;
  }
  LDSBAR();
  TS(1, 1);
  const unsigned so = (unsigned)WQKVT_OFF + (hq * 14 + 2 * w) * 18 * 1024;
  const int g = lane >> 4, c = lane & 15;
  f32x4 acc[2][4];
  if (w == 7) {
    qkv_body<false, false, false>(rs, vo, so, xv, hb, modv, w, lane, acc PROFPASS);
    const int i = blockIdx.x * 64 + lane;
    if (i < 864) {
      const f32x4 v = mod4((const float*)(ws + MODP_OFF), 4 * i);
      const int vec = i / 144;
      *(f32x4*)(ws + MODF_OFF + 16 * i) = (vec == 1 || vec == 4) ? gain4(v) : v;
    }
    TS(1, 4); TSEND(1);
    return;
  }
  if (w < 4) qkv_body<true, false, false>(rs, vo, so, xv, hb, modv, w, lane, acc PROFPASS);
  else if (w < 6) qkv_body<true, true, true>(rs, vo, so, xv, hb, modv, w, lane, acc PROFPASS);
  else qkv_body<true, false, true>(rs, vo, so, xv, hb, modv, w, lane, acc PROFPASS);
  TS(1, 4);
  const float sc = w < 2 || (w == 6 && g < 2) ? QSCALE : 1.f;
  unsigned o[2][4][2];
#pragma unroll
  for (int j = 0; j < 2; ++j) {
    const float f = (w == 6 && j == 1) ? 1.f : sc;
#pragma unroll
    for (int t = 0; t < 4; ++t) {
      o[j][t][0] = cvtpk(acc[j][t][0] * f, acc[j][t][1] * f);
      o[j][t][1] = cvtpk(acc[j][t][2] * f, acc[j][t][3] * f);
    }
  }
  const unsigned kvo = (unsigned)KV_OFF + rb * 2 * KVT;
  if (w < 4) {
#pragma unroll
    for (int j = 0; j < 2; ++j) {
      const int h = 4 * hq + 2 * (w & 1) + j;
      const unsigned base = w < 2 ? (unsigned)QLO_OFF + (h * 64 + rb * 2) * 1024 : kvo + h * KVH, st = w < 2 ? 1024 : KVT;
#pragma unroll
      for (int p = 0; p < 2; ++p) {
        swap16(o[j][2 * p][0], o[j][2 * p + 1][0]); swap16(o[j][2 * p][1], o[j][2 * p + 1][1]);
        bst16<AUX_K1>(rs, vo, base + p * st, u32x4{o[j][2 * p][0], o[j][2 * p][1], o[j][2 * p + 1][0], o[j][2 * p + 1][1]});
      }
    }
  } else {
#pragma unroll
    for (int j = 0; j < 2; ++j) {
      unsigned vv;
      unsigned base = kvo + 1152;
      if (w < 6) { base += (4 * hq + 2 * (w & 1) + j) * KVH; vv = c * 16; }
      else if (j == 0) continue;
      else {
        const int h = c < 8 ? c >> 1 : c & 3, d = c < 8 ? 16 + (c & 1) : c < 12 ? 18 : 19;
        vv = (4 * hq + h) * KVH + d * 16;
        if (c >= 8) {
          const unsigned cv = c < 12 ? 0x3F803F80u : 0u;
#pragma unroll
          for (int t = 0; t < 4; ++t) { o[1][t][0] = cv; o[1][t][1] = cv; }
        }
      }
      vv += (g >> 1) * 640 + (g & 1) * 320;
#pragma unroll
      for (int p = 0; p < 2; ++p) {
        swap32(o[j][2 * p][0], o[j][2 * p + 1][0]); swap32(o[j][2 * p][1], o[j][2 * p + 1][1]);
        bst16<AUX_K1>(rs, vv, base + p * KVT, u32x4{o[j][2 * p][0], o[j][2 * p][1], o[j][2 * p + 1][0], o[j][2 * p + 1][1]});
      }
    }
    if (w == 6) {
#pragma unroll
      for (int e = 0; e < 2; ++e) {
        const int h = 4 * hq + 2 * (g & 1) + e;
        const unsigned v0 = g < 2 ? (unsigned)QHI_OFF + (h * 2048 + rb * 64 + c) * 4 : kvo + h * KVH + 1024 + c * 4;
        const unsigned v1 = v0 + (g < 2 ? 128 : KVT);
#pragma unroll
        for (int t = 0; t < 4; ++t) bst4<AUX_K1>(rs, (t < 2 ? v0 : v1) + (t & 1) * 64, 0, o[0][t][e]);
      }
    }
  }
  TS(1, 5); TSEND(1);
}

#define ATT_ORDER 0
#define ATT_VAR 0
DI float ex2(float x) {
  return __builtin_amdgcn_exp2f(x);
}
typedef short s16x4v __attribute__((ext_vector_type(4)));
DI f32x16 mfma32k8(u32x4 a, u32x4 b, f32x16 c) {
  return mfma32(a, b, c);
}
DI f32x16 mfmapv(u32x4 a, u32x4 b, f32x16 c) {
  return mfma32(a, b, c);
}
DI u32x4 ldsv(const LAS char* p, u32x4 old, bool act) {
  return *(const LAS u32x4*)p;
}
#define ATT_G(P, i0, S, j0) \
  e0 = ex2(S[j0]); e1 = ex2(S[j0 + 1]); e2 = ex2(S[j0 + 2]); e3 = ex2(S[j0 + 3]); \
  SBAR(); P[i0] = cvtpk(e0, e1); P[i0 + 1] = cvtpk(e2, e3); SBAR();
DI void attn_step(const LAS char* kp, const LAS char* hp, const LAS char* vp, int off, u32x4& ka, u32x4& kb, u32x4& v0, u32x4& v1,
                  const u32x4 q0, const u32x4 q1, const f32x16& cin, f32x16& sc, f32x16& sn, u32x4& p1, f32x16& o, bool vact) {
  u32x4 p0;
  float e0, e1, e2, e3;
  ATT_G(p0, 0, sc, 0)
  o = mfmapv(v1, p1, o);
  SBAR();
  v1 = ldsv(vp + off + 640, v1, vact);
  ATT_G(p0, 2, sc, 4)
  sn = mfma32k8(kb, q1, sn);
  SBAR();
  kb[0] = *(const LAS unsigned*)(hp + off + 2 * KVT);
  ATT_G(p1, 0, sc, 8)
  o = mfmapv(v0, p0, o);
  SBAR();
  v0 = ldsv(vp + off + KVT, v0, vact);
  ATT_G(p1, 2, sc, 12)
  sc = mfma32(ka, q0, cin);
  SBAR();
  ka = lds128(kp + off + 3 * KVT);
}
template <bool SUBMAX, bool STAGED>
DI void attn_all(const LAS char* kv, u32x4 q0, u32x4 q1, float negm, int lane, f32x16& o) {
  const LAS char* kp = kv + lane * 16;
  const LAS char* hp = kv + 1024 + (lane & 31) * 4;
  const LAS char* vp = kv + 1152 + (lane >> 5) * 320 + (lane & 31) * 16;
  f32x16 cin;
#pragma unroll
  for (int i = 0; i < 16; ++i) cin[i] = SUBMAX ? negm : 0.f;
  u32x4 ka = lds128(kp);
  u32x4 kb = {*(const LAS unsigned*)(hp), 0u, 0u, 0u};
  f32x16 sa = mfma32(ka, q0, cin), sb;
  sa = mfma32k8(kb, q1, sa);
  ka = lds128(kp + KVT);
  sb = mfma32(ka, q0, cin);
  ka = lds128(kp + 2 * KVT);
  kb[0] = *(const LAS unsigned*)(hp + KVT);
  const bool vact = (lane & 31) < 20;
  const u32x4 zz = {0u, 0u, 0u, 0u};
  u32x4 v0 = ldsv(vp, zz, vact), v1 = zz, p1 = zz;
#pragma unroll 1
  for (int c = 0; c < 8; ++c) {
    if (STAGED) {
      if (c == 1 || c == 2) asm volatile("s_barrier" ::: "memory");
    }
#pragma unroll
    for (int u = 0; u < 8; u += 2) {
      attn_step(kp, hp, vp, u * KVT, ka, kb, v0, v1, q0, q1, cin, sa, sb, p1, o, vact);
      attn_step(kp, hp, vp, (u + 1) * KVT, ka, kb, v0, v1, q0, q1, cin, sb, sa, p1, o, vact);
    }
    kp += 8 * KVT; hp += 8 * KVT; vp += 8 * KVT;
  }
  o = mfmapv(v1, p1, o);
}
#define HELP_PRIO 0
#define HELP_MODE 2
template <int NU, bool ISW1>
DI void retile_units(const u16* __restrict__ w1, const u16* __restrict__ w2, const u16* __restrict__ wo, char* __restrict__ ws,
                     LAS char* stage, int job, int lane) {
  if (!ISW1) __builtin_amdgcn_s_setprio(2);
  u32x4 raw[NU][4];
  const int rr = lane >> 3, cc = (lane & 7) * 8;
#pragma unroll
  for (int u = 0; u < NU; ++u) {
    if (ISW1) {
      const int cgp = job / 6, s = (job % 6) * 3 + u;
#pragma unroll
      for (int i = 0; i < 4; ++i) raw[u][i] = *(const u32x4*)(w1 + (size_t)(32 * s + 8 * i + rr) * F + cgp * 64 + cc);
    } else {
      const int un = 3 * job + u;
      const bool isw2 = un < 1296;
      const int sg = un % 144, v = un - 1296, s = v % 18;
      const int n0 = isw2 ? (un / 144) * 64 : (v / 18) * 64;
#pragma unroll
      for (int i = 0; i < 4; ++i) {
        const int r = (8 * i + rr) & 15, g = r >> 2, j = 4 * (i >> 1) + (r & 3), ci = 4 * s + g, G = ci / 9, r9 = ci % 9;
        const int ko = r9 < 8 ? (4 * G + (r9 >> 1)) * 18 + 8 * (r9 & 1) + j : (4 * G + (j >> 1)) * 18 + 16 + (j & 1);
        const int k = isw2 ? 32 * sg + 8 * i + rr : ko;
        raw[u][i] = *(const u32x4*)((isw2 ? w2 : wo) + (size_t)k * D + n0 + cc);
      }
    }
  }
  SBAR();
  const int g = lane >> 4, c = lane & 15;
  u32x4 bv[ISW1 ? NU : 1];
  if (ISW1) {
#pragma unroll
    for (int u = 0; u < NU; ++u) {
      const float* mp = (const float*)(ws + MODF_OFF) + ((c & 1) ? 3 : 4) * D + 32 * ((job % 6) * 3 + u) + 8 * g;
      const f32x4 lo = *(const f32x4*)mp, hi = *(const f32x4*)(mp + 4);
      bv[u] = c < 2 ? u32x4{cvtpk(lo[0], lo[1]), cvtpk(lo[2], lo[3]), cvtpk(hi[0], hi[1]), cvtpk(hi[2], hi[3])} : u32x4{0u, 0u, 0u, 0u};
    }
  }
  const rsrc_t rs = ws_rsrc(ws);
  f32x4 acc[4];
#pragma unroll
  for (int i = 0; i < 4; ++i) acc[i] = f32x4{0.f, 0.f, 0.f, 0.f};
  const LAS char* rp = stage + (4 * g + (c >> 2)) * 128 + (lane & 3) * 8;
#pragma unroll
  for (int u = 0; u < NU; ++u) {
    u32x2 lo[4], hi[4];
    LAS char* sp = stage + (rr & 3) * 128 + (lane & 7) * 16;
    if (ISW1) {
      if (rr < 4) {
#pragma unroll
        for (int i = 0; i < 4; ++i) *(LAS u32x4*)(sp + i * 512) = raw[u][i];
      }
    } else {
      *(LAS u32x4*)(stage + lane * 16) = raw[u][0];
      *(LAS u32x4*)(stage + 1024 + lane * 16) = raw[u][1];
    }
#pragma unroll
    for (int i = 0; i < 4; ++i) lo[i] = tr_read(rp + i * 32);
    asm volatile("s_waitcnt lgkmcnt(0)" ::: "memory");
    if (ISW1) {
      if (rr >= 4) {
#pragma unroll
        for (int i = 0; i < 4; ++i) *(LAS u32x4*)(sp + i * 512) = raw[u][i];
      }
    } else {
      *(LAS u32x4*)(stage + lane * 16) = raw[u][2];
      *(LAS u32x4*)(stage + 1024 + lane * 16) = raw[u][3];
    }
#pragma unroll
    for (int i = 0; i < 4; ++i) hi[i] = tr_read(rp + i * 32);
    asm volatile("s_waitcnt lgkmcnt(0)" ::: "memory");
#pragma unroll
    for (int i = 0; i < 4; ++i) {
      const u32x4 fragv = {lo[i][0], lo[i][1], hi[i][0], hi[i][1]};
      size_t off;
      if (ISW1) {
        const int Tn = (job / 6) * 4 + i, s = (job % 6) * 3 + u;
        off = W1T_OFF + ((size_t)(Tn / 36) * 648 + frag_index1(Tn % 36, s)) * 1024;
        acc[i] = mfma16(fragv, bv[u], acc[i]);
      } else {
        const int un = 3 * job + u;
        if (un < 1296) {
          const int sg = un % 144;
          off = W2T_OFF + ((size_t)(sg / 18) * 648 + frag_index((un / 144) * 4 + i, sg % 18)) * 1024;
        } else {
          const int v = un - 1296;
          off = WOT_OFF + ((size_t)((v / 18) * 4 + i) * 18 + v % 18) * 1024;
        }
      }
      bst16<AUX_RT>(rs, lane * 16, (unsigned)off, fragv);
    }
    if (u == 0 && !ISW1) __builtin_amdgcn_s_setprio(0);
  }
  if (ISW1 && c < 2) {
#pragma unroll
    for (int i = 0; i < 4; ++i)
      *(f32x4*)(ws + CSB_OFF + ((size_t)((job % 6) * 2 + c) * F + ((job / 6) * 4 + i) * 16 + 4 * g) * 4) = acc[i];
  }
}
__global__ void __launch_bounds__(768, 3) k2_attn(const u16* __restrict__ w1, const u16* __restrict__ w2, const u16* __restrict__ wo,
                                                  char* __restrict__ ws) {
  __shared__ __attribute__((aligned(16))) char lds[163840];
  const int tid = threadIdx.x, lane = tid & 63, w = __builtin_amdgcn_readfirstlane(tid >> 6);
  const int xcd = blockIdx.x & 7, idx = blockIdx.x >> 3, h = 4 * xcd + (idx & 3), qb = idx >> 2;
  TSDECL; TS(2, 0);
  if (w >= 8) {
    __builtin_amdgcn_s_setprio(HELP_PRIO);
    {
      const char* src = ws + KV_OFF + (size_t)h * KVH + (w - 8) * 2048 + lane * 16;
      const unsigned lb = (unsigned)(size_t)(LAS char*)lds + (w - 8) * 2048;
      asm volatile("s_barrier" ::: "memory");
#pragma unroll
      for (int r = 4; r < 12; ++r) { glds16(src + r * 8192, lb + r * 8192); glds16(src + r * 8192 + 1024, lb + r * 8192 + 1024); }
      asm volatile("s_waitcnt vmcnt(12)\n\ts_barrier" ::: "memory");
#pragma unroll
      for (int r = 12; r < 19; ++r) { glds16(src + r * 8192, lb + r * 8192); glds16(src + r * 8192 + 1024, lb + r * 8192 + 1024); }
      asm volatile("s_waitcnt vmcnt(0)\n\ts_barrier" ::: "memory");
    }
    TS(2, 1);
    LAS char* stage = (LAS char*)lds + KVH + (w - 8) * 2048;
    const int job = blockIdx.x * 2 + (w & 1);
    if (w < 10) { if (job < 432) retile_units<3, true>(w1, w2, wo, ws, stage, job, lane); }
    else if (job < 486) retile_units<3, false>(w1, w2, wo, ws, stage, job, lane);
    TS(2, 2); TSEND(2);
    return;
  }
  const int qt = qb * 8 + w;
  u32x4 q0 = *(const u32x4*)(ws + QLO_OFF + ((size_t)h * 64 + qt) * 1024 + lane * 16);
  unsigned qh = *(const unsigned*)(ws + QHI_OFF + ((size_t)h * 2048 + qt * 32 + (lane & 31)) * 4);
  const char* src = ws + KV_OFF + (size_t)h * KVH + w * 1024 + lane * 16;
  const unsigned lb = (unsigned)(size_t)(LAS char*)lds + w * 1024;
#pragma unroll
  for (int r = 0; r < 4; ++r) glds16(src + r * 8192, lb + r * 8192);
  asm volatile("" : "+v"(q0), "+v"(qh));
  WAIT_VM(0);
  const u32x4 q1 = {lane < 32 ? qh : 0u, 0u, 0u, 0u};
  const LAS char* kv = (const LAS char*)lds;
  f32x16 o;
#pragma unroll
  for (int i = 0; i < 16; ++i) o[i] = 0.f;
  asm volatile("s_barrier" ::: "memory");
  TS(2, 1);
  attn_all<false, true>(kv, q0, q1, 0.f, lane, o);
  TS(2, 5);
  float l = __shfl(o[10], lane & 31);
  bool bad = !(l >= 7.8886091e-31f && l <= 1.3292280e36f);
#pragma unroll
  for (int i = 0; i < 10; ++i) bad |= !(__builtin_fabsf(o[i]) <= 3.0e38f) && (i < 8 || lane < 32);
  if (__builtin_expect(__any(bad), 0) && ATT_VAR < 3) {
    float m = -3.0e38f;
    const LAS char* kp = kv + lane * 16;
    const LAS char* hp = kv + 1024 + (lane & 31) * 4;
    f32x16 z;
#pragma unroll
    for (int i = 0; i < 16; ++i) z[i] = 0.f;
#pragma unroll 1
    for (int t = 0; t < 64; ++t) {
      const u32x4 ka = lds128(kp + t * KVT);
      const u32x4 kb = {*(const LAS unsigned*)(hp + t * KVT), 0u, 0u, 0u};
      f32x16 sc = mfma32(ka, q0, z);
      sc = mfma32(kb, q1, sc);
#pragma unroll
      for (int i = 0; i < 16; ++i) m = __builtin_fmaxf(m, sc[i]);
    }
    m = __builtin_fmaxf(m, __shfl_xor(m, 32));
#pragma unroll
    for (int i = 0; i < 16; ++i) o[i] = 0.f;
    attn_all<true, false>(kv, q0, q1, -m, lane, o);
    l = __shfl(o[10], lane & 31);
  }
  const float rl = 1.f / l;
  const int hf = lane >> 5, ci = 9 * (h >> 2) + 2 * (h & 3) + hf, ch = 9 * (h >> 2) + 8;
  const unsigned tokoff = ((lane >> 4) & 1) * 1024 + (lane & 15) * 16, ob = (unsigned)OF_OFF + qt * 36864;
  unsigned a0 = cvtpk(o[0] * rl, o[1] * rl), a1 = cvtpk(o[2] * rl, o[3] * rl), b0 = cvtpk(o[4] * rl, o[5] * rl), b1 = cvtpk(o[6] * rl, o[7] * rl);
  swap32(a0, b0); swap32(a1, b1);
  const rsrc_t rs = ws_rsrc(ws);
  bst16<AUX_O>(rs, (ci >> 2) * 2048 + (ci & 3) * 256 + tokoff, ob, u32x4{a0, a1, b0, b1});
  if (lane < 32) bst4<0>(rs, (ch >> 2) * 2048 + (ch & 3) * 256 + tokoff + (h & 3) * 4, ob, cvtpk(o[8] * rl, o[9] * rl));
  TS(2, 6); TSEND(2);
}

#define AUX_K3 16
#define AUX_K3G 16
#define K3_WIN 12
__global__ void __launch_bounds__(576) k3_wo(const u16* __restrict__ x, char* __restrict__ ws) {
  __shared__ __attribute__((aligned(16))) char lds[36 * 1024 + 9 * 64 * 4];
  const int tid = threadIdx.x, lane = tid & 63, w = __builtin_amdgcn_readfirstlane(tid >> 6);
  const int rb = (blockIdx.x & 7) * 8 + ((blockIdx.x >> 3) & 7), cg = blockIdx.x >> 6;
  TSDECL; TS(3, 0);
  const int g = lane >> 4, c = lane & 15;
  u32x4 cs[6];
  if (w == 8) {
    const rsrc_t rs = ws_rsrc(ws);
#pragma unroll
    for (int k = 0; k < 6; ++k) cs[k] = bld16(rs, lane < 9 ? (blockIdx.x * 9 + lane) * 16 : OOB, (unsigned)CSB_OFF + k * 2 * F * 4);
  }
  const unsigned lb = (unsigned)(size_t)(LAS char*)lds;
#pragma unroll
  for (int i = 0; i < 4; ++i) glds16(ws + OF_OFF + (size_t)rb * 36864 + (w * 4 + i) * 1024 + lane * 16, lb + (w * 4 + i) * 1024);
  if (blockIdx.x < 32 && tid == 448) *(unsigned*)(ws + CNT_OFF + blockIdx.x * 128) = 0u;
  const int T = cg * 9 + w;
  const u32x4* ap = (const u32x4*)(ws + WOT_OFF + (size_t)T * 18 * 1024) + lane;
  constexpr int W3 = K3_WIN;
  u32x4 a[18];
  SBAR();
#pragma unroll
  for (int s = 0; s < W3; ++s) a[s] = ap[s * 64];
  SBAR();
  asm volatile("s_waitcnt vmcnt(%0)\n\ts_barrier" :: "i"(W3) : "memory");
  TS(3, 1);
  if (w == 8 && lane < 9) {
    f32x4 sm = __builtin_bit_cast(f32x4, cs[0]);
#pragma unroll
    for (int k = 1; k < 6; ++k) sm += __builtin_bit_cast(f32x4, cs[k]);
    *(f32x4*)(ws + CSBF_OFF + (blockIdx.x * 9 + lane) * 16) = sm * -1.4426950408889634f;
  }
  u32x2 xr[2];
  f32x4 ga, gb2;
  f32x4 acc[2] = {f32x4{0.f, 0.f, 0.f, 0.f}, f32x4{0.f, 0.f, 0.f, 0.f}};
  const LAS char* bp = (const LAS char*)lds + lane * 16;
#pragma unroll
  for (int s = 0; s < 18; ++s) {
    if (s + W3 < 18) a[s + W3] = ap[(s + W3) * 64];
    if (s == 12) {
#pragma unroll
      for (int t = 0; t < 2; ++t) xr[t] = *(const u32x2*)(x + (size_t)(rb * 32 + t * 16 + c) * D + T * 16 + 4 * g);
    }
    if (s == 13) ga = *(const f32x4*)(ws + MODF_OFF + (2 * D + T * 16 + 4 * g) * 4);
    if (s == 14) gb2 = *(const f32x4*)(ws + MODF_OFF + (4 * D + T * 16 + 4 * g) * 4);
    const u32x4 b0 = lds128(bp + (s * 2) * 1024), b1 = lds128(bp + (s * 2 + 1) * 1024);
    SBAR();
    acc[0] = mfma16(a[s], b0, acc[0]);
    acc[1] = mfma16(a[s], b1, acc[1]);
    SBAR();
  }
  TS(3, 3);
  u32x4 xo;
  unsigned og[2][2];
  const rsrc_t rs3 = ws_rsrc(ws);
  LAS float* red = (LAS float*)(lds + 36 * 1024);
#pragma unroll
  for (int t = 0; t < 2; ++t) {
    const unsigned o0 = cvtpk(bflo(xr[t][0]) + ga[0] * acc[t][0], bfhi(xr[t][0]) + ga[1] * acc[t][1]);
    const unsigned o1 = cvtpk(bflo(xr[t][1]) + ga[2] * acc[t][2], bfhi(xr[t][1]) + ga[3] * acc[t][3]);
    xo[2 * t] = o0; xo[2 * t + 1] = o1;
    const float v0 = bflo(o0), v1 = bfhi(o0), v2 = bflo(o1), v3 = bfhi(o1);
    og[t][0] = cvtpk(v0 * gb2[0], v1 * gb2[1]); og[t][1] = cvtpk(v2 * gb2[2], v3 * gb2[3]);
    float s1 = (v0 + v1) + (v2 + v3), s2 = (v0 * v0 + v1 * v1) + (v2 * v2 + v3 * v3);
    s1 += __shfl_xor(s1, 16); s2 += __shfl_xor(s2, 16);
    s1 += __shfl_xor(s1, 32); s2 += __shfl_xor(s2, 32);
    if (g == 0) { red[w * 64 + t * 16 + c] = s1; red[w * 64 + 32 + t * 16 + c] = s2; }
  }
  bst16<AUX_K3>(rs3, lane * 16, (unsigned)X1_OFF + (((rb >> 1) * 36 + T) * 2 + (rb & 1)) * 1024, xo);
  swap16(og[0][0], og[1][0]); swap16(og[0][1], og[1][1]);
  bst16<AUX_K3G>(rs3, (g & 1) * 1024 + ((2 * (T & 1) + (g >> 1)) * 16 + c) * 16,
                 (unsigned)X1G_OFF + (rb >> 1) * 73728 + ((T >> 1) * 4 + (rb & 1) * 2) * 1024, u32x4{og[0][0], og[0][1], og[1][0], og[1][1]});
  asm volatile("s_waitcnt lgkmcnt(0)\n\ts_barrier" ::: "memory");
  if (tid < 64) {
    float sm = 0.f;
#pragma unroll
    for (int i = 0; i < 9; ++i) sm += red[i * 64 + tid];
    ((float*)(ws + STAT_OFF))[(rb * 4 + cg) * 64 + tid] = sm;
  }
  TS(3, 2); TSEND(3);
}

#define K4_X 0
#define K4_WIN 8
struct Silu { float z[4], e[4]; unsigned p[2]; };
constexpr int SOPS = 27;
#define PIN(x) asm volatile("" : "+v"(x))
template <int k>
DI void silu_op(Silu& q, const f32x4& v, const f32x4& cs, const f32x4& bs, float rs, float nm, LAS char* dst) {
  if constexpr (k < 4) { q.z[k] = __builtin_fmaf(cs[k], nm, bs[k]); PIN(q.z[k]); }
  else if constexpr (k < 8) { q.z[k - 4] = __builtin_fmaf(v[k - 4], rs, q.z[k - 4]); PIN(q.z[k - 4]); }
  else if constexpr (k < 12) { q.e[k - 8] = __builtin_amdgcn_exp2f(q.z[k - 8]); PIN(q.e[k - 8]); }
  else if constexpr (k < 16) { q.e[k - 12] = 1.f + q.e[k - 12]; PIN(q.e[k - 12]); }
  else if constexpr (k < 20) { q.e[k - 16] = __builtin_amdgcn_rcpf(q.e[k - 16]); PIN(q.e[k - 16]); }
  else if constexpr (k < 24) { q.e[k - 20] = q.z[k - 20] * q.e[k - 20]; PIN(q.e[k - 20]); }
  else if constexpr (k < 26) { q.p[k - 24] = cvtpk(q.e[2 * (k - 24)], q.e[2 * (k - 24) + 1]); PIN(q.p[k - 24]); }
  else *(LAS u32x2*)dst = u32x2{q.p[0], q.p[1]};
}
template <int NT>
DI void mlp_body(char* __restrict__ ws, u16* __restrict__ out, LAS char* hb, LAS char* h1, LAS float* st, LAS float* csb, int w, int lane, int fr, int rb,
                 f32x4 sv0, f32x4 sv1 PROFARG) {
  constexpr int NB = NT - 2, NF = NT * KS, WIN = K4_WIN;
  const int T0 = NT == 5 ? 5 * w : 20 + 4 * (w - 4);
  const int TA = 2 * w, TB = NT == 5 ? 16 + 3 * w : 28 + 2 * (w - 4);
  const unsigned wbase = (unsigned)(fr * 648 + (NT == 5 ? w * 90 : 360 + (w - 4) * 72)) * 1024u;
  const rsrc_t rs_ = ws_rsrc(ws);
  const unsigned vo = lane * 16;
  const unsigned o1 = (unsigned)W1T_OFF + wbase, o2 = (unsigned)W2T_OFF + wbase;
#define K4_EXP 0
#define WLOAD(i) bld16(rs_, vo, (i) < NF ? o1 + (i) * 1024u : o2 + ((i) - NF) * 1024u)
  u32x4 a[WIN];
#pragma unroll
  for (int i = 0; i < WIN; ++i) a[i] = WLOAD(i);
  SBAR();
  constexpr float NL2E = -1.4426950408889634f;
  {
    const int tid = w * 64 + lane;
    if (tid < 64) {
      const float s1 = (sv0[0] + sv0[1]) + (sv0[2] + sv0[3]), s2 = (sv1[0] + sv1[1]) + (sv1[2] + sv1[3]);
      const float mean = s1 * (1.f / D);
      const float var = __builtin_fmaxf(s2 * (1.f / D) - mean * mean, 0.f);
      const float rstd = __builtin_amdgcn_rsqf(var + 1e-5f);
      st[2 * tid] = rstd * NL2E; st[2 * tid + 1] = -mean * rstd;
    } else if (tid < 352) {
      *(LAS f32x4*)(csb + 4 * (tid - 64)) = sv0;
    }
  }
  asm volatile("s_waitcnt vmcnt(%0) lgkmcnt(0)\n\ts_barrier" :: "i"(WIN) : "memory");
  TS(4, 2);
  unsigned bp0 = (unsigned)(size_t)hb + lane * 16, bp1 = bp0 + 40960, hp0 = (unsigned)(size_t)h1 + lane * 16, hp1 = hp0 + 40960;
  asm volatile("" : "+v"(bp0), "+v"(bp1), "+v"(hp0), "+v"(hp1));
#define XFRAG(f) lds128((const LAS char*)(size_t)((f) < 40 ? bp0 + (f) * 1024 : bp1 + ((f) - 40) * 1024))
#define HFRAG(f) lds128((const LAS char*)(size_t)((f) < 40 ? hp0 + (f) * 1024 : hp1 + ((f) - 40) * 1024))
  const int g = lane >> 4, c = lane & 15;
  f32x4 accA[2][4], accB[NB][4], acc[NT][4];
  u32x4 bf[2][4];
  const f32x4 z4 = {0.f, 0.f, 0.f, 0.f};
#pragma unroll
  for (int t = 0; t < 4; ++t) bf[0][t] = XFRAG(t);
#pragma unroll
  for (int s = 0; s < KS; ++s) {
#pragma unroll
    for (int t = 0; t < 4; ++t) bf[(s + 1) & 1][t] = XFRAG(((s + 1) % KS) * 4 + t);
    SBAR();
#pragma unroll
    for (int j = 0; j < 2; ++j) {
      const int i = s * 2 + j;
#pragma unroll
      for (int t = 0; t < 4; ++t) accA[j][t] = mfma16(a[i % WIN], bf[s & 1][t], s == 0 ? z4 : accA[j][t]);
      SBAR();
      a[i % WIN] = WLOAD(i + WIN);
      SBAR();
    }
  }
  TS(4, 3);
  float rs[4], nm[4];
#pragma unroll
  for (int t = 0; t < 4; ++t) { rs[t] = st[2 * (t * 16 + c)]; nm[t] = st[2 * (t * 16 + c) + 1]; }
  const LAS float* csg = csb + 4 * g;
  f32x4 cs[2], bs[2];
  cs[0] = *(const LAS f32x4*)(csg + TA * 16); bs[0] = *(const LAS f32x4*)(csg + 576 + TA * 16);
  cs[1] = *(const LAS f32x4*)(csg + TA * 16 + 16); bs[1] = *(const LAS f32x4*)(csg + 576 + TA * 16 + 16);
  Silu sq;
  sfor<0, KS>(LAM(s_) {
    constexpr int s = s_.v, M = 16 * NB * 4, TOT = 8 * SOPS;
    if constexpr (s + 1 < KS) {
#pragma unroll
      for (int t = 0; t < 4; ++t) bf[(s + 1) & 1][t] = XFRAG((s + 1) * 4 + t);
    }
    SBAR();
    sfor<0, NB>(LAM(j_) {
      constexpr int j = j_.v, i = 36 + s * NB + j;
      sfor<0, 4>(LAM(t_) {
        constexpr int t = t_.v, m = (s * NB + j) * 4 + t;
        accB[j][t] = mfma16(a[i % WIN], bf[s & 1][t], s == 0 ? z4 : accB[j][t]);
        PIN(accB[j][t]);
        if constexpr (m < M) {
          sfor<m * TOT / M, (m + 1) * TOT / M>(LAM(o_) {
            constexpr int o = o_.v, q = o / SOPS, jj = q >> 2, tt = q & 3;
            silu_op<o % SOPS>(sq, accA[jj][tt], cs[jj], bs[jj], rs[tt], nm[tt], h1 + (w * 4 + tt) * 1024 + lane * 16 + jj * 8);
          });
        }
        SBAR();
      });
      a[i % WIN] = WLOAD(i + WIN);
      SBAR();
    });
  });
  TS(4, 4);
  cs[0] = *(const LAS f32x4*)(csg + TB * 16); bs[0] = *(const LAS f32x4*)(csg + 576 + TB * 16);
  cs[1] = *(const LAS f32x4*)(csg + TB * 16 + 16); bs[1] = *(const LAS f32x4*)(csg + 576 + TB * 16 + 16);
  asm volatile("s_waitcnt lgkmcnt(0)\n\ts_barrier" ::: "memory");
  TS(4, 5);
#pragma unroll
  for (int t = 0; t < 4; ++t) bf[0][t] = HFRAG(t);
  sfor<0, 8>(LAM(s_) {
    constexpr int s = s_.v, M = 8 * NT * 4, TOT = NB * 4 * SOPS;
    if constexpr (s + 1 < 8) {
#pragma unroll
      for (int t = 0; t < 4; ++t) bf[(s + 1) & 1][t] = HFRAG((s + 1) * 4 + t);
    }
    SBAR();
    sfor<0, NT>(LAM(j_) {
      constexpr int j = j_.v, i = NF + s * NT + j;
      sfor<0, 4>(LAM(t_) {
        constexpr int t = t_.v, m = (s * NT + j) * 4 + t;
        acc[j][t] = mfma16(a[i % WIN], bf[s & 1][t], s == 0 ? z4 : acc[j][t]);
        PIN(acc[j][t]);
        sfor<m * TOT / M, (m + 1) * TOT / M>(LAM(o_) {
          constexpr int o = o_.v, q = o / SOPS, jj = q >> 2, tt = q & 3;
          const int T = TB + jj;
          if constexpr (NB == 3 && o == 4 * SOPS + 8) {
            cs[0] = *(const LAS f32x4*)(csg + TB * 16 + 32); bs[0] = *(const LAS f32x4*)(csg + 576 + TB * 16 + 32);
          }
          silu_op<o % SOPS>(sq, accB[jj][tt], cs[jj & 1], bs[jj & 1], rs[tt], nm[tt], h1 + ((T >> 1) * 4 + tt) * 1024 + lane * 16 + (T & 1) * 8);
        });
        SBAR();
      });
      a[i % WIN] = WLOAD(i + WIN);
      SBAR();
    });
  });
  TS(4, 6);
  asm volatile("s_waitcnt lgkmcnt(0)\n\ts_barrier" ::: "memory");
  TS(4, 7);
#pragma unroll
  for (int t = 0; t < 4; ++t) bf[0][t] = HFRAG(32 + t);
#pragma unroll
  for (int s = 8; s < KS; ++s) {
    if (s + 1 < KS) {
#pragma unroll
      for (int t = 0; t < 4; ++t) bf[(s + 1) & 1][t] = HFRAG((s + 1) * 4 + t);
    }
    SBAR();
#pragma unroll
    for (int j = 0; j < NT; ++j) {
      const int i = NF + s * NT + j;
#pragma unroll
      for (int t = 0; t < 4; ++t) acc[j][t] = mfma16(a[i % WIN], bf[s & 1][t], acc[j][t]);
      SBAR();
      if (i + WIN < 2 * NF) a[i % WIN] = WLOAD(i + WIN);
      SBAR();
    }
  }
  TS(4, 8);
#pragma unroll
  for (int j = 0; j < NT; ++j) {
    const int T = T0 + j;
#pragma unroll
    for (int tp = 0; tp < 2; ++tp) {
      const f32x4 v0 = acc[j][2 * tp], v1 = acc[j][2 * tp + 1];
      u32x4 o = {cvtpk(v0[0], v0[1]), cvtpk(v0[2], v0[3]), cvtpk(v1[0], v1[1]), cvtpk(v1[2], v1[3])};
      bst16<AUX_PART>(rs_, vo, (unsigned)(PART_OFF + ((((size_t)fr * 32 + rb) * 36 + T) * 2 + tp) * 1024), o);
    }
  }
  TS(4, 9);
  const int u0 = fr * 9 + w;
  u32x4 xr = bld16(rs_, vo, (unsigned)X1_OFF + (rb * 72 + u0) * 1024), xr2;
  f32x4 gb = *(const f32x4*)(ws + MODF_OFF + (5 * D + (u0 >> 1) * 16 + 4 * g) * 4), gb2;
  if (w == 0) {
    xr2 = bld16(rs_, vo, (unsigned)X1_OFF + (rb * 72 + u0 + 8) * 1024);
    gb2 = *(const f32x4*)(ws + MODF_OFF + (5 * D + ((u0 + 8) >> 1) * 16 + 4 * g) * 4);
  }
  asm volatile("s_waitcnt vmcnt(0)\n\ts_barrier" ::: "memory");
  TS(4, 10);
  if (w == 0) {
    GAS unsigned* cnt = (GAS unsigned*)(ws + CNT_OFF + rb * 128);
    if (lane == 0) __hip_atomic_fetch_add(cnt, 1u, __ATOMIC_RELAXED, __HIP_MEMORY_SCOPE_AGENT);
    for (unsigned spins = 0; spins < (1u << 20); ++spins) {
      if (__hip_atomic_load(cnt, __ATOMIC_RELAXED, __HIP_MEMORY_SCOPE_AGENT) >= 8u) break;
      __builtin_amdgcn_s_sleep(1);
    }
  }
  asm volatile("s_barrier" ::: "memory");
  TS(4, 11);
  auto reduce = [&](int unit, const u32x4& xv, const f32x4& gv) __attribute__((always_inline)) {
    u32x4 p[8];
#pragma unroll
    for (int f = 0; f < 8; ++f)
      p[f] = __builtin_amdgcn_raw_buffer_load_b128(rs_, vo, (unsigned)PART_OFF + ((f * 32 + rb) * 72 + unit) * 1024, 16);
    const f32x4 gm = gv * -0.6931471805599453f;
    const int col = (unit >> 1) * 16 + 4 * g;
#pragma unroll
    for (int t = 0; t < 2; ++t) {
      float a[4] = {0.f, 0.f, 0.f, 0.f};
#pragma unroll
      for (int f = 0; f < 8; ++f) {
        a[0] += bflo(p[f][2 * t]); a[1] += bfhi(p[f][2 * t]);
        a[2] += bflo(p[f][2 * t + 1]); a[3] += bfhi(p[f][2 * t + 1]);
      }
      u32x2 o = {cvtpk(bflo(xv[2 * t]) + gm[0] * a[0], bfhi(xv[2 * t]) + gm[1] * a[1]),
                 cvtpk(bflo(xv[2 * t + 1]) + gm[2] * a[2], bfhi(xv[2 * t + 1]) + gm[3] * a[3])};
      *(u32x2*)(out + (size_t)(rb * 64 + (unit & 1) * 32 + t * 16 + c) * D + col) = o;
    }
  };
  if (w == 0) {
    u32x4 p2[8];
#pragma unroll
    for (int f = 0; f < 8; ++f)
      p2[f] = __builtin_amdgcn_raw_buffer_load_b128(rs_, vo, (unsigned)PART_OFF + ((f * 32 + rb) * 72 + u0 + 8) * 1024, 16);
    SBAR();
    reduce(u0, xr, gb);
    const f32x4 gm = gb2 * -0.6931471805599453f;
    const int unit = u0 + 8, col = (unit >> 1) * 16 + 4 * g;
#pragma unroll
    for (int t = 0; t < 2; ++t) {
      float a[4] = {0.f, 0.f, 0.f, 0.f};
#pragma unroll
      for (int f = 0; f < 8; ++f) {
        a[0] += bflo(p2[f][2 * t]); a[1] += bfhi(p2[f][2 * t]);
        a[2] += bflo(p2[f][2 * t + 1]); a[3] += bfhi(p2[f][2 * t + 1]);
      }
      u32x2 o = {cvtpk(bflo(xr2[2 * t]) + gm[0] * a[0], bfhi(xr2[2 * t]) + gm[1] * a[1]),
                 cvtpk(bflo(xr2[2 * t + 1]) + gm[2] * a[2], bfhi(xr2[2 * t + 1]) + gm[3] * a[3])};
      *(u32x2*)(out + (size_t)(rb * 64 + (unit & 1) * 32 + t * 16 + c) * D + col) = o;
    }
  } else {
    reduce(u0, xr, gb);
  }
  TS(4, 12); TSEND(4);
#undef WLOAD
#undef XFRAG
#undef HFRAG
}
#define K4_MAP 2
__global__ void __launch_bounds__(512, 2) k4_mlp(char* __restrict__ ws, u16* __restrict__ out) {
  __shared__ __attribute__((aligned(16))) char lds[2 * 73728 + 512 + 4608];
  const int tid = threadIdx.x, lane = tid & 63, w = __builtin_amdgcn_readfirstlane(tid >> 6);
  const int xcd = blockIdx.x & 7, idx = blockIdx.x >> 3;
  const int rb = (xcd >> 1) * 8 + (idx & 7), fr = (xcd & 1) * 4 + (idx >> 3);
  LAS char* hb = (LAS char*)lds;
  LAS char* h1 = hb + 73728;
  LAS float* st = (LAS float*)(hb + 2 * 73728);
  LAS float* csb = st + 128;
  TSDECL; TS(4, 0);
  f32x4 sv0 = {0.f, 0.f, 0.f, 0.f}, sv1 = sv0;
  if (w == 0) {
    const float* sp = (const float*)(ws + STAT_OFF) + (size_t)(rb * 2 + (tid >> 5)) * 256 + (tid & 31);
    sv0 = f32x4{sp[0], sp[64], sp[128], sp[192]};
    sv1 = f32x4{sp[32], sp[96], sp[160], sp[224]};
  } else {
    const int i = tid < 352 ? tid - 64 : 0;
    sv0 = *(const f32x4*)(ws + CSBF_OFF + ((size_t)(i >= 144) * F + fr * 576 + 4 * (i >= 144 ? i - 144 : i)) * 4);
  }
  const char* src = ws + X1G_OFF + (size_t)rb * 73728 + w * 9216 + lane * 16;
  const unsigned lb = (unsigned)(size_t)hb + w * 9216;
#pragma unroll
  for (int i = 0; i < 9; ++i) glds16(src + i * 1024, lb + i * 1024);
  if (w < 4) mlp_body<5>(ws, out, hb, h1, st, csb, w, lane, fr, rb, sv0, sv1 PROFPASS);
  else mlp_body<4>(ws, out, hb, h1, st, csb, w, lane, fr, rb, sv0, sv1 PROFPASS);
}

extern "C" void kernel_launch(void* const* d_in, const int* in_sizes, int n_in, void* d_out, int out_size, void* d_ws,
                              size_t ws_size, hipStream_t stream) {
  const u16* x = (const u16*)d_in[0];
  const u16* wqkv = (const u16*)d_in[1];
  const u16* wo = (const u16*)d_in[2];
  const u16* w1 = (const u16*)d_in[4];
  const u16* w2 = (const u16*)d_in[5];
  const u16* c = (const u16*)d_in[6];
  const u16* wmod = (const u16*)d_in[7];
  char* ws = (char*)d_ws;
  k0_prep<<<K0_MOD + K0_LN + K0_QKV_BLKS, 256, 0, stream>>>(c, wmod, wqkv, x, ws);
  k1_qkv<<<256, 512, 0, stream>>>(ws);
  k2_attn<<<256, 768, 0, stream>>>(w1, w2, wo, ws);
  k3_wo<<<256, 576, 0, stream>>>(x, ws);
  k4_mlp<<<256, 512, 0, stream>>>(ws, (u16*)d_out);
}
```

```cpp
#include <hip/hip_runtime.h>
#include <hip/hip_bf16.h>
#include <stdint.h>

typedef unsigned short u16;
typedef short bf16x8 __attribute__((ext_vector_type(8)));
typedef short s16x4 __attribute__((ext_vector_type(4)));
typedef float f32x4 __attribute__((ext_vector_type(4)));
typedef float f32x16 __attribute__((ext_vector_type(16)));
typedef unsigned u32x4 __attribute__((ext_vector_type(4)));
typedef unsigned u32x2 __attribute__((ext_vector_type(2)));
#define LAS __attribute__((address_space(3)))
#define GAS __attribute__((address_space(1)))
#define DI __device__ __forceinline__

constexpr int S = 2048, D = 576, NH = 32, DH = 18, F = 4608;
constexpr int KS = 18;

constexpr size_t MODP_OFF = 0;
constexpr size_t WQKVT_OFF = 0x10000;
constexpr size_t WOT_OFF = WQKVT_OFF + 8 * 14 * 18 * 1024;
constexpr size_t W1T_OFF = WOT_OFF + 36 * 18 * 1024;
constexpr size_t W2T_OFF = W1T_OFF + 8 * 648 * 1024;
constexpr size_t QLO_OFF = W2T_OFF + 8 * 648 * 1024;
constexpr size_t QHI_OFF = QLO_OFF + 32 * 64 * 1024;
constexpr int KVT = 2432;
constexpr int KVH = 64 * KVT;
constexpr size_t KV_OFF = QHI_OFF + 32 * 2048 * 4;
constexpr size_t OF_OFF = KV_OFF + (size_t)32 * KVH;
constexpr size_t X1_OFF = OF_OFF + (size_t)S * D * 2;
constexpr size_t X1G_OFF = X1_OFF + (size_t)S * D * 2;
constexpr size_t NF_OFF = X1G_OFF + (size_t)S * D * 2;
constexpr size_t STAT_OFF = NF_OFF + (size_t)S * D * 2;
 constexpr size_t CSB_OFF = STAT_OFF + 32 * 2 * 64 * 8 * 4;
constexpr size_t CSBF_OFF = CSB_OFF + 6 * 2 * 4608 * 4;
constexpr size_t MODF_OFF = CSBF_OFF + 2 * 4608 * 4;
constexpr size_t CNT_OFF = MODF_OFF + 6 * D * 4;
constexpr size_t PART_OFF = CSB_OFF + 0x50000;
constexpr size_t WS_END = PART_OFF + (size_t)8 * S * D * 2;

#define TSDECL
#define TS(kid, i)
#define TSEND(kid)
#define PROFARG
#define PROFPASS
template <int I> struct IC { static constexpr int v = I; };
template <int B, int E, class Fn> __device__ __forceinline__ void sfor(Fn&& f) {
  if constexpr (B < E) { f(IC<B>{}); sfor<B + 1, E>(f); }
}
#define LAM(x) [&](auto x) __attribute__((always_inline))
DI float bf2f(unsigned v) { return __builtin_bit_cast(float, v << 16); }
DI float bflo(unsigned v) { return __builtin_bit_cast(float, v << 16); }
DI float bfhi(unsigned v) { return __builtin_bit_cast(float, v & 0xffff0000u); }
typedef __bf16 bf16x2_t __attribute__((ext_vector_type(2)));
typedef float f32x2 __attribute__((ext_vector_type(2)));
DI unsigned cvtpk(float lo, float hi) {
  return __builtin_bit_cast(unsigned, __builtin_convertvector(f32x2{lo, hi}, bf16x2_t));
}
DI float rbf(float x) { return bflo(cvtpk(x, x)); }
DI f32x4 mfma16(u32x4 a, u32x4 b, f32x4 c) {
  return __builtin_amdgcn_mfma_f32_16x16x32_bf16(__builtin_bit_cast(bf16x8, a), __builtin_bit_cast(bf16x8, b), c, 0, 0, 0);
}
DI f32x16 mfma32(u32x4 a, u32x4 b, f32x16 c) {
  return __builtin_amdgcn_mfma_f32_32x32x16_bf16(__builtin_bit_cast(bf16x8, a), __builtin_bit_cast(bf16x8, b), c, 0, 0, 0);
}
DI void glds16(const void* g, unsigned lds_base) {
  unsigned sv;
  asm volatile("s_mov_b32 %0, m0\n\ts_mov_b32 m0, %2\n\ts_nop 0\n\tglobal_load_lds_dwordx4 %1, off\n\ts_mov_b32 m0, %0"
               : "=&s"(sv) : "v"(g), "s"(lds_base) : "memory");
}
typedef __amdgpu_buffer_rsrc_t rsrc_t;
DI rsrc_t ws_rsrc(const char* ws) { return __builtin_amdgcn_make_buffer_rsrc((void*)ws, (short)0, (int)WS_END, 0x00020000); }
template <int AUX> DI void bst16(rsrc_t r, unsigned voff, unsigned soff, u32x4 v) { __builtin_amdgcn_raw_buffer_store_b128(v, r, voff + soff, 0, AUX); }
template <int AUX> DI void bst8(rsrc_t r, unsigned voff, unsigned soff, u32x2 v) { __builtin_amdgcn_raw_buffer_store_b64(v, r, voff, soff, AUX); }
template <int AUX> DI void bst4(rsrc_t r, unsigned voff, unsigned soff, unsigned v) { __builtin_amdgcn_raw_buffer_store_b32(v, r, voff, soff, AUX); }
DI u32x4 bld16(rsrc_t r, unsigned voff, unsigned soff) { return __builtin_amdgcn_raw_buffer_load_b128(r, voff, soff, 0); }
#define AUX_RT 16
#define AUX_K0L 16
#define AUX_K0G 16
#define AUX_O 16
#define AUX_PART 16
#define WAIT_VM(n) asm volatile("s_waitcnt vmcnt(" #n ")" ::: "memory")
#define SBAR() __builtin_amdgcn_sched_barrier(0)
DI float silu_f(float x) { return x * __builtin_amdgcn_rcpf(1.f + __builtin_amdgcn_exp2f(-1.4426950408889634f * x)); }
typedef short v4i16_t __attribute__((ext_vector_type(4)));
DI u32x2 tr_read(const LAS char* p) {
  return __builtin_bit_cast(u32x2, __builtin_amdgcn_ds_read_tr16_b64_v4i16((LAS v4i16_t*)p));
}
template <int CTRL> DI unsigned dppu(unsigned v) { return __builtin_amdgcn_update_dpp(0u, v, CTRL, 0xF, 0xF, true); }
DI unsigned dpp_xor1(unsigned v) { return dppu<0xB1>(v); }
template <int CTRL> DI float dppf(float v) { return __builtin_bit_cast(float, dppu<CTRL>(__builtin_bit_cast(unsigned, v))); }
DI float sum16(float v) { v += dppf<0xB1>(v); v += dppf<0x4E>(v); v += dppf<0x141>(v); v += dppf<0x140>(v); return v; }
DI u32x4 lds128(const LAS char* p) { return *(const LAS u32x4*)p; }

DI int frag_index(int T, int s) {
  return T < 20 ? (T / 5) * 90 + s * 5 + (T % 5) : 360 + ((T - 20) >> 2) * 72 + s * 4 + ((T - 20) & 3);
}

DI int frag_index1(int T, int s) {
  if (T < 16) { const int w = T >> 1; return (w < 4 ? 90 * w : 360 + 72 * (w - 4)) + 2 * s + (T & 1); }
  if (T < 28) { const int w = (T - 16) / 3; return 90 * w + 36 + 3 * s + (T - 16) % 3; }
  const int w = 4 + ((T - 28) >> 1);
  return 360 + 72 * (w - 4) + 36 + 2 * s + (T & 1);
}

constexpr int K0_MOD = 216, K0_LN = 128, K0_QKV_BLKS = 144;
__global__ void __launch_bounds__(256) k0_prep(const u16* __restrict__ c, const u16* __restrict__ wmod, const u16* __restrict__ wqkv,
                                               const u16* __restrict__ x, char* __restrict__ ws) {
  __shared__ __attribute__((aligned(16))) char lds[18432];
  const int tid = threadIdx.x, lane = tid & 63, w = __builtin_amdgcn_readfirstlane(tid >> 6);
  int b = blockIdx.x;
  TSDECL; TS(0, 0);
  if (b < K0_MOD) {
    const int cc = b % 54, ks = b / 54;
    const int ch = tid & 7, rg = tid >> 3;
    float acc[8];
#pragma unroll
    for (int j = 0; j < 8; ++j) acc[j] = 0.f;
    u32x4 wv[5];
    float sv[5];
    unsigned cr[5];
#pragma unroll
    for (int i = 0; i < 5; ++i) {
      const int r = rg + 32 * i, k = ks * 144 + (r < 144 ? r : 0);
      cr[i] = c[k];
      wv[i] = *(const u32x4*)(wmod + (size_t)k * 3456 + cc * 64 + ch * 8);
    }
    SBAR();
#pragma unroll
    for (int i = 0; i < 5; ++i) sv[i] = rbf(silu_f(bf2f(cr[i]))) * (rg + 32 * i < 144 ? 1.f : 0.f);
#pragma unroll
    for (int i = 0; i < 5; ++i)
#pragma unroll
      for (int j = 0; j < 4; ++j) {
        acc[2 * j] += sv[i] * bflo(wv[i][j]);
        acc[2 * j + 1] += sv[i] * bfhi(wv[i][j]);
      }
#pragma unroll
    for (int j = 0; j < 8; ++j) {
      acc[j] += __shfl_xor(acc[j], 8);
      acc[j] += __shfl_xor(acc[j], 16);
      acc[j] += __shfl_xor(acc[j], 32);
    }
    float* red = (float*)lds;
    if (lane < 8)
#pragma unroll
      for (int j = 0; j < 8; ++j) red[w * 64 + lane * 8 + j] = acc[j];
    __syncthreads();
    if (tid < 64) ((float*)(ws + MODP_OFF))[ks * 3456 + cc * 64 + tid] = red[tid] + red[64 + tid] + red[128 + tid] + red[192 + tid];
    TS(0, 1); TSEND(0);
    return;
  }
  b -= K0_MOD;
  if (b < K0_LN) {
    const int tok = b * 16 + w * 4 + (lane >> 4), p = lane & 15;
    const u16* xp = x + (size_t)tok * D + 4 * p;
    u32x2 xv[9];
#pragma unroll
    for (int i = 0; i < 9; ++i) xv[i] = *(const u32x2*)(xp + 64 * i);
    float sm = 0.f;
#pragma unroll
    for (int i = 0; i < 9; ++i) sm += (bflo(xv[i][0]) + bfhi(xv[i][0])) + (bflo(xv[i][1]) + bfhi(xv[i][1]));
    const float mean = sum16(sm) * (1.f / D);
    float q = 0.f;
#pragma unroll
    for (int i = 0; i < 9; ++i)
#pragma unroll
      for (int j = 0; j < 2; ++j) { float a0 = bflo(xv[i][j]) - mean, a1 = bfhi(xv[i][j]) - mean; q += a0 * a0; q += a1 * a1; }
    const float rstd = __builtin_amdgcn_rsqf(sum16(q) * (1.f / D) + 1e-5f);
    LAS char* dst = (LAS char*)lds + (p >> 3) * 1024 + ((p >> 1) & 3) * 256 + (tok & 15) * 16 + (p & 1) * 8;
#pragma unroll
    for (int i = 0; i < 9; ++i) {
      u32x2 o = {cvtpk((bflo(xv[i][0]) - mean) * rstd, (bfhi(xv[i][0]) - mean) * rstd),
                 cvtpk((bflo(xv[i][1]) - mean) * rstd, (bfhi(xv[i][1]) - mean) * rstd)};
      *(LAS u32x2*)(dst + i * 2048) = o;
    }
    __syncthreads();
    const rsrc_t rs0 = ws_rsrc(ws);
#pragma unroll
    for (int i = 0; i < 5; ++i) {
      const int s = w + 4 * i;
      if (s < 18) bst16<AUX_K0L>(rs0, lane * 16, (unsigned)NF_OFF + (b >> 2) * 73728 + s * 4096 + (b & 3) * 1024, lds128((const LAS char*)lds + s * 1024 + lane * 16));
    }
    TS(0, 1); TSEND(0);
    return;
  }
  b -= K0_LN;
  {
    const int s = b % 18, hq = b / 18;
    constexpr int PITCH = 448;
    u32x4 raw[4];
#pragma unroll
    for (int i = 0; i < 4; ++i) {
      const int e = min(tid + 256 * i, 863), row = e / 27, pc = e % 27;
      raw[i] = *(const u32x4*)(wqkv + (size_t)(32 * s + row) * 1728 + (pc / 9) * 576 + hq * 72 + (pc % 9) * 8);
    }
#pragma unroll
    for (int i = 0; i < 4; ++i) {
      const int e = min(tid + 256 * i, 863), row = e / 27, pc = e % 27;
      *(LAS u32x4*)((LAS char*)lds + row * PITCH + pc * 16) = raw[i];
    }
    __syncthreads();
    const int g = lane >> 4, r = lane & 15;
    const rsrc_t rs0 = ws_rsrc(ws);
#pragma unroll
    for (int i = 0; i < 4; ++i) {
      const int tile = w + 4 * i;
      if (tile < 14) {
        int col; bool ok = true;
        if (tile < 12) col = (tile >> 2) * 72 + (tile & 3) * 18 + r;
        else if (tile == 12) col = (r >> 3) * 72 + ((r >> 1) & 3) * 18 + 16 + (r & 1);
        else { ok = r < 8; col = 144 + ((r >> 1) & 3) * 18 + 16 + (r & 1); }
        const LAS u16* p = (const LAS u16*)((LAS char*)lds + 8 * g * PITCH) + col;
        u32x4 o;
#pragma unroll
        for (int j = 0; j < 4; ++j) o[j] = ok ? ((unsigned)p[(2 * j) * (PITCH / 2)] | ((unsigned)p[(2 * j + 1) * (PITCH / 2)] << 16)) : 0u;
        bst16<AUX_K0G>(rs0, lane * 16, (unsigned)WQKVT_OFF + ((hq * 14 + tile) * 18 + s) * 1024, o);
      }
    }
    TS(0, 1); TSEND(0);
  }
}

DI f32x4 mod4(const float* __restrict__ modp, int col) {
  const f32x4 a = *(const f32x4*)(modp + col), b = *(const f32x4*)(modp + 3456 + col), c = *(const f32x4*)(modp + 2 * 3456 + col),
              d = *(const f32x4*)(modp + 3 * 3456 + col);
  return f32x4{rbf(a[0] + b[0] + c[0] + d[0]), rbf(a[1] + b[1] + c[1] + d[1]), rbf(a[2] + b[2] + c[2] + d[2]), rbf(a[3] + b[3] + c[3] + d[3])};
}
DI f32x4 gain4(f32x4 v) { return f32x4{rbf(1.f + v[0]), rbf(1.f + v[1]), rbf(1.f + v[2]), rbf(1.f + v[3])}; }

constexpr float QSCALE = 0.23570226039551584f * 1.4426950408889634f;
DI u32x4 modulate8(u32x4 v, const f32x4& g0, const f32x4& g1, const f32x4& s0, const f32x4& s1) {
  return u32x4{cvtpk(bflo(v[0]) * g0[0] + s0[0], bfhi(v[0]) * g0[1] + s0[1]), cvtpk(bflo(v[1]) * g0[2] + s0[2], bfhi(v[1]) * g0[3] + s0[3]),
               cvtpk(bflo(v[2]) * g1[0] + s1[0], bfhi(v[2]) * g1[1] + s1[1]), cvtpk(bflo(v[3]) * g1[2] + s1[2], bfhi(v[3]) * g1[3] + s1[3])};
}
#define AUX_K1 16
#define RAWBAR() asm volatile("s_barrier" ::: "memory")
#define LDSBAR() asm volatile("s_waitcnt lgkmcnt(0)\n\ts_barrier" ::: "memory")
DI void swap16(unsigned& a, unsigned& b) { const auto r = __builtin_amdgcn_permlane16_swap(a, b, false, false); a = r[0]; b = r[1]; }
DI void swap32(unsigned& a, unsigned& b) { const auto r = __builtin_amdgcn_permlane32_swap(a, b, false, false); a = r[0]; b = r[1]; }
DI float sum_rows(float v) {
  unsigned a = __builtin_bit_cast(unsigned, v), b = a;
  swap16(a, b);
  a = __builtin_bit_cast(unsigned, __builtin_bit_cast(float, a) + __builtin_bit_cast(float, b)); b = a;
  swap32(a, b);
  return __builtin_bit_cast(float, a) + __builtin_bit_cast(float, b);
}
constexpr int K1_WIN = 12;
constexpr unsigned OOB = 0x40000000u;
template <bool N0, bool N1>
DI void qkv_gemm(rsrc_t rs, unsigned vo, unsigned so, u32x4 (&a)[K1_WIN], const LAS char* b0, f32x4 (&acc)[2][4]) {
  constexpr int WIN = K1_WIN;
  const f32x4 z4 = {0.f, 0.f, 0.f, 0.f};
#pragma unroll
  for (int j = 0; j < 2; ++j)
#pragma unroll
    for (int t = 0; t < 4; ++t) acc[j][t] = z4;
  u32x4 bf[2][4];
#pragma unroll
  for (int t = 0; t < 4; ++t) bf[0][t] = lds128(b0 + t * 1024);
#pragma unroll 1
  for (int it = 0; it < 3; ++it) {
    const unsigned vn = it < 2 ? vo : OOB;
#pragma unroll
    for (int u = 0; u < 6; ++u) {
      const LAS char* bn = b0 + (u + 1) * 4096;
#pragma unroll
      for (int t = 0; t < 4; ++t) bf[(u + 1) & 1][t] = lds128(bn + t * 1024);
      SBAR();
#pragma unroll
      for (int j = 0; j < 2; ++j) {
#pragma unroll
        for (int t = 0; t < 4; ++t)
          acc[j][t] = (j == 0 ? N0 : N1) ? mfma16(bf[u & 1][t], a[2 * u + j], acc[j][t]) : mfma16(a[2 * u + j], bf[u & 1][t], acc[j][t]);
        SBAR();
        a[2 * u + j] = bld16(rs, vn, so + (j * 18 + 6 + u) * 1024);
        SBAR();
      }
    }
    b0 += 6 * 4096; so += 6 * 1024;
  }
}
template <bool GEMM, bool N0, bool N1>
DI void qkv_body(rsrc_t rs, unsigned vo, unsigned so, const u32x4 (&xv)[9], LAS char* hb, const LAS float* modv, int w, int lane,
                 f32x4 (&acc)[2][4] PROFARG) {
  u32x4 a[K1_WIN];
  const LAS float* gp = modv + (lane >> 4) * 8;
  f32x4 gs[3][4];
#pragma unroll
  for (int k = 0; k < 3; ++k) {
    const LAS float* p = gp + (k < 2 ? 2 * w + k : 16 + (w >> 2)) * 32;
    gs[k][0] = *(const LAS f32x4*)p; gs[k][1] = *(const LAS f32x4*)(p + 4);
    gs[k][2] = *(const LAS f32x4*)(p + 576); gs[k][3] = *(const LAS f32x4*)(p + 580);
  }
  SBAR();
  sfor<0, 9>(LAM(i_) {
    constexpr int i = i_.v, l0 = i < 3 ? 2 * i : i + 3, l1 = i < 3 ? 2 * i + 2 : i + 4;
    if constexpr (GEMM) sfor<l0, l1>(LAM(l_) { constexpr int l = l_.v; a[l] = bld16(rs, vo, so + ((l & 1) * 18 + (l >> 1)) * 1024); });
    SBAR();
    const u32x4 m = modulate8(xv[i], gs[i >> 2][0], gs[i >> 2][1], gs[i >> 2][2], gs[i >> 2][3]);
    *(LAS u32x4*)(hb + (i < 8 ? 8 * w + i : 64 + w) * 1024 + lane * 16) = m;
    SBAR();
  });
  TS(1, 2);
  LDSBAR();
  TS(1, 3);
  if constexpr (GEMM) qkv_gemm<N0, N1>(rs, vo, so, a, hb + lane * 16, acc);
}
__global__ void __launch_bounds__(512, 2) k1_qkv(char* __restrict__ ws) {
  __shared__ __attribute__((aligned(16))) char lds[73728 + 4608];
  const int tid = threadIdx.x, lane = tid & 63, w = __builtin_amdgcn_readfirstlane(tid >> 6);
  const int xcd = blockIdx.x & 7, idx = blockIdx.x >> 3;
  const int rb = (xcd >> 1) * 8 + (idx & 7), hq = (xcd & 1) * 4 + (idx >> 3);
  LAS char* hb = (LAS char*)lds;
  LAS float* modv = (LAS float*)(hb + 73728);
  const rsrc_t rs = ws_rsrc(ws);
  const unsigned vo = lane * 16;
  TSDECL; TS(1, 0);
  const bool domod = tid < 288;
  const unsigned mo = domod ? (unsigned)MODP_OFF + (tid < 144 ? D + 4 * tid : 4 * (tid - 144)) * 4 : OOB;
  u32x4 mp[4];
#pragma unroll
  for (int i = 0; i < 4; ++i) mp[i] = bld16(rs, mo, i * 3456 * 4);
  TS(1, 6);
  SBAR(); RAWBAR(); SBAR();
  TS(1, 7);
  u32x4 xv[9];
  {
    const unsigned no = (unsigned)NF_OFF + rb * 73728;
#pragma unroll
    for (int i = 0; i < 8; ++i) xv[i] = bld16(rs, vo, no + (8 * w + i) * 1024);
    xv[8] = bld16(rs, vo, no + (64 + w) * 1024);
  }
  SBAR();
  TS(1, 8);
  {
    const f32x4 ms = __builtin_bit_cast(f32x4, mp[0]) + __builtin_bit_cast(f32x4, mp[1]) + __builtin_bit_cast(f32x4, mp[2]) + __builtin_bit_cast(f32x4, mp[3]);
    const f32x4 mv = {rbf(ms[0]), rbf(ms[1]), rbf(ms[2]), rbf(ms[3])};
    *(LAS f32x4*)(domod ? (LAS char*)(modv + 4 * tid) : hb + 16 * tid) = tid < 144 ? gain4(mv) : mv;
  }
  LDSBAR();
  TS(1, 1);
  const unsigned so = (unsigned)WQKVT_OFF + (hq * 14 + 2 * w) * 18 * 1024;
  const int g = lane >> 4, c = lane & 15;
  f32x4 acc[2][4];
  if (w == 7) {
    qkv_body<false, false, false>(rs, vo, so, xv, hb, modv, w, lane, acc PROFPASS);
    const int i = blockIdx.x * 64 + lane;
    if (i < 864) {
      const f32x4 v = mod4((const float*)(ws + MODP_OFF), 4 * i);
      const int vec = i / 144;
      *(f32x4*)(ws + MODF_OFF + 16 * i) = (vec == 1 || vec == 4) ? gain4(v) : v;
    }
    if (blockIdx.x >= 64 && blockIdx.x < 128 && lane == 0) *(unsigned*)(ws + CNT_OFF + (blockIdx.x - 64) * 128) = 0u;
    TS(1, 4); TSEND(1);
    return;
  }
  if (w < 4) qkv_body<true, false, false>(rs, vo, so, xv, hb, modv, w, lane, acc PROFPASS);
  else if (w < 6) qkv_body<true, true, true>(rs, vo, so, xv, hb, modv, w, lane, acc PROFPASS);
  else qkv_body<true, false, true>(rs, vo, so, xv, hb, modv, w, lane, acc PROFPASS);
  TS(1, 4);
  const float sc = w < 2 || (w == 6 && g < 2) ? QSCALE : 1.f;
  unsigned o[2][4][2];
#pragma unroll
  for (int j = 0; j < 2; ++j) {
    const float f = (w == 6 && j == 1) ? 1.f : sc;
#pragma unroll
    for (int t = 0; t < 4; ++t) {
      o[j][t][0] = cvtpk(acc[j][t][0] * f, acc[j][t][1] * f);
      o[j][t][1] = cvtpk(acc[j][t][2] * f, acc[j][t][3] * f);
    }
  }
  const unsigned kvo = (unsigned)KV_OFF + rb * 2 * KVT;
  if (w < 4) {
#pragma unroll
    for (int j = 0; j < 2; ++j) {
      const int h = 4 * hq + 2 * (w & 1) + j;
      const unsigned base = w < 2 ? (unsigned)QLO_OFF + (h * 64 + rb * 2) * 1024 : kvo + h * KVH, st = w < 2 ? 1024 : KVT;
#pragma unroll
      for (int p = 0; p < 2; ++p) {
        swap16(o[j][2 * p][0], o[j][2 * p + 1][0]); swap16(o[j][2 * p][1], o[j][2 * p + 1][1]);
        bst16<AUX_K1>(rs, vo, base + p * st, u32x4{o[j][2 * p][0], o[j][2 * p][1], o[j][2 * p + 1][0], o[j][2 * p + 1][1]});
      }
    }
  } else {
#pragma unroll
    for (int j = 0; j < 2; ++j) {
      unsigned vv;
      unsigned base = kvo + 1152;
      if (w < 6) { base += (4 * hq + 2 * (w & 1) + j) * KVH; vv = c * 16; }
      else if (j == 0) continue;
      else {
        const int h = c < 8 ? c >> 1 : c & 3, d = c < 8 ? 16 + (c & 1) : c < 12 ? 18 : 19;
        vv = (4 * hq + h) * KVH + d * 16;
        if (c >= 8) {
          const unsigned cv = c < 12 ? 0x3F803F80u : 0u;
#pragma unroll
          for (int t = 0; t < 4; ++t) { o[1][t][0] = cv; o[1][t][1] = cv; }
        }
      }
      vv += (g >> 1) * 640 + (g & 1) * 320;
#pragma unroll
      for (int p = 0; p < 2; ++p) {
        swap32(o[j][2 * p][0], o[j][2 * p + 1][0]); swap32(o[j][2 * p][1], o[j][2 * p + 1][1]);
        bst16<AUX_K1>(rs, vv, base + p * KVT, u32x4{o[j][2 * p][0], o[j][2 * p][1], o[j][2 * p + 1][0], o[j][2 * p + 1][1]});
      }
    }
    if (w == 6) {
#pragma unroll
      for (int e = 0; e < 2; ++e) {
        const int h = 4 * hq + 2 * (g & 1) + e;
        const unsigned v0 = g < 2 ? (unsigned)QHI_OFF + (h * 2048 + rb * 64 + c) * 4 : kvo + h * KVH + 1024 + c * 4;
        const unsigned v1 = v0 + (g < 2 ? 128 : KVT);
#pragma unroll
        for (int t = 0; t < 4; ++t) bst4<AUX_K1>(rs, (t < 2 ? v0 : v1) + (t & 1) * 64, 0, o[0][t][e]);
      }
    }
  }
  TS(1, 5); TSEND(1);
}

#define ATT_ORDER 0
#define ATT_VAR 0
DI float ex2(float x) {
  return __builtin_amdgcn_exp2f(x);
}
typedef short s16x4v __attribute__((ext_vector_type(4)));
DI f32x16 mfma32k8(u32x4 a, u32x4 b, f32x16 c) {
  return mfma32(a, b, c);
}
DI f32x16 mfmapv(u32x4 a, u32x4 b, f32x16 c) {
  return mfma32(a, b, c);
}
DI u32x4 ldsv(const LAS char* p, u32x4 old, bool act) {
  return *(const LAS u32x4*)p;
}
#define ATT_G(P, i0, S, j0) \
  e0 = ex2(S[j0]); e1 = ex2(S[j0 + 1]); e2 = ex2(S[j0 + 2]); e3 = ex2(S[j0 + 3]); \
  SBAR(); P[i0] = cvtpk(e0, e1); P[i0 + 1] = cvtpk(e2, e3); SBAR();
DI void attn_step(const LAS char* kp, const LAS char* hp, const LAS char* vp, int off, u32x4& ka, u32x4& kb, u32x4& v0, u32x4& v1,
                  const u32x4 q0, const u32x4 q1, const f32x16& cin, f32x16& sc, f32x16& sn, u32x4& p1, f32x16& o, bool vact) {
  u32x4 p0;
  float e0, e1, e2, e3;
  ATT_G(p0, 0, sc, 0)
  o = mfmapv(v1, p1, o);
  SBAR();
  v1 = ldsv(vp + off + 640, v1, vact);
  ATT_G(p0, 2, sc, 4)
  sn = mfma32k8(kb, q1, sn);
  SBAR();
  kb[0] = *(const LAS unsigned*)(hp + off + 2 * KVT);
  ATT_G(p1, 0, sc, 8)
  o = mfmapv(v0, p0, o);
  SBAR();
  v0 = ldsv(vp + off + KVT, v0, vact);
  ATT_G(p1, 2, sc, 12)
  sc = mfma32(ka, q0, cin);
  SBAR();
  ka = lds128(kp + off + 3 * KVT);
}
template <bool SUBMAX, bool STAGED>
DI void attn_all(const LAS char* kv, u32x4 q0, u32x4 q1, float negm, int lane, f32x16& o) {
  const LAS char* kp = kv + lane * 16;
  const LAS char* hp = kv + 1024 + (lane & 31) * 4;
  const LAS char* vp = kv + 1152 + (lane >> 5) * 320 + (lane & 31) * 16;
  f32x16 cin;
#pragma unroll
  for (int i = 0; i < 16; ++i) cin[i] = SUBMAX ? negm : 0.f;
  u32x4 ka = lds128(kp);
  u32x4 kb = {*(const LAS unsigned*)(hp), 0u, 0u, 0u};
  f32x16 sa = mfma32(ka, q0, cin), sb;
  sa = mfma32k8(kb, q1, sa);
  ka = lds128(kp + KVT);
  sb = mfma32(ka, q0, cin);
  ka = lds128(kp + 2 * KVT);
  kb[0] = *(const LAS unsigned*)(hp + KVT);
  const bool vact = (lane & 31) < 20;
  const u32x4 zz = {0u, 0u, 0u, 0u};
  u32x4 v0 = ldsv(vp, zz, vact), v1 = zz, p1 = zz;
#pragma unroll 1
  for (int c = 0; c < 8; ++c) {
    if (STAGED) {
      if (c == 1 || c == 2) asm volatile("s_barrier" ::: "memory");
    }
#pragma unroll
    for (int u = 0; u < 8; u += 2) {
      attn_step(kp, hp, vp, u * KVT, ka, kb, v0, v1, q0, q1, cin, sa, sb, p1, o, vact);
      attn_step(kp, hp, vp, (u + 1) * KVT, ka, kb, v0, v1, q0, q1, cin, sb, sa, p1, o, vact);
    }
    kp += 8 * KVT; hp += 8 * KVT; vp += 8 * KVT;
  }
  o = mfmapv(v1, p1, o);
}
#define HELP_PRIO 0
#define HELP_MODE 2
template <int NU, bool ISW1>
DI void retile_units(const u16* __restrict__ w1, const u16* __restrict__ w2, const u16* __restrict__ wo, char* __restrict__ ws,
                     LAS char* stage, int job, int lane) {
  if (!ISW1) __builtin_amdgcn_s_setprio(2);
  u32x4 raw[NU][4];
  const int rr = lane >> 3, cc = (lane & 7) * 8;
#pragma unroll
  for (int u = 0; u < NU; ++u) {
    if (ISW1) {
      const int cgp = job / 6, s = (job % 6) * 3 + u;
#pragma unroll
      for (int i = 0; i < 4; ++i) raw[u][i] = *(const u32x4*)(w1 + (size_t)(32 * s + 8 * i + rr) * F + cgp * 64 + cc);
    } else {
      const int un = 3 * job + u;
      const bool isw2 = un < 1296;
      const int sg = un % 144, v = un - 1296, s = v % 18;
      const int n0 = isw2 ? (un / 144) * 64 : (v / 18) * 64;
#pragma unroll
      for (int i = 0; i < 4; ++i) {
        const int r = (8 * i + rr) & 15, g = r >> 2, j = 4 * (i >> 1) + (r & 3), ci = 4 * s + g, G = ci / 9, r9 = ci % 9;
        const int ko = r9 < 8 ? (4 * G + (r9 >> 1)) * 18 + 8 * (r9 & 1) + j : (4 * G + (j >> 1)) * 18 + 16 + (j & 1);
        const int k = isw2 ? 32 * sg + 8 * i + rr : ko;
        raw[u][i] = *(const u32x4*)((isw2 ? w2 : wo) + (size_t)k * D + n0 + cc);
      }
    }
  }
  SBAR();
  const int g = lane >> 4, c = lane & 15;
  u32x4 bv[ISW1 ? NU : 1];
  if (ISW1) {
#pragma unroll
    for (int u = 0; u < NU; ++u) {
      const float* mp = (const float*)(ws + MODF_OFF) + ((c & 1) ? 3 : 4) * D + 32 * ((job % 6) * 3 + u) + 8 * g;
      const f32x4 lo = *(const f32x4*)mp, hi = *(const f32x4*)(mp + 4);
      bv[u] = c < 2 ? u32x4{cvtpk(lo[0], lo[1]), cvtpk(lo[2], lo[3]), cvtpk(hi[0], hi[1]), cvtpk(hi[2], hi[3])} : u32x4{0u, 0u, 0u, 0u};
    }
  }
  const rsrc_t rs = ws_rsrc(ws);
  f32x4 acc[4];
#pragma unroll
  for (int i = 0; i < 4; ++i) acc[i] = f32x4{0.f, 0.f, 0.f, 0.f};
  const LAS char* rp = stage + (4 * g + (c >> 2)) * 128 + (lane & 3) * 8;
#pragma unroll
  for (int u = 0; u < NU; ++u) {
    u32x2 lo[4], hi[4];
    LAS char* sp = stage + (rr & 3) * 128 + (lane & 7) * 16;
    if (ISW1) {
      if (rr < 4) {
#pragma unroll
        for (int i = 0; i < 4; ++i) *(LAS u32x4*)(sp + i * 512) = raw[u][i];
      }
    } else {
      *(LAS u32x4*)(stage + lane * 16) = raw[u][0];
      *(LAS u32x4*)(stage + 1024 + lane * 16) = raw[u][1];
    }
#pragma unroll
    for (int i = 0; i < 4; ++i) lo[i] = tr_read(rp + i * 32);
    asm volatile("s_waitcnt lgkmcnt(0)" ::: "memory");
    if (ISW1) {
      if (rr >= 4) {
#pragma unroll
        for (int i = 0; i < 4; ++i) *(LAS u32x4*)(sp + i * 512) = raw[u][i];
      }
    } else {
      *(LAS u32x4*)(stage + lane * 16) = raw[u][2];
      *(LAS u32x4*)(stage + 1024 + lane * 16) = raw[u][3];
    }
#pragma unroll
    for (int i = 0; i < 4; ++i) hi[i] = tr_read(rp + i * 32);
    asm volatile("s_waitcnt lgkmcnt(0)" ::: "memory");
#pragma unroll
    for (int i = 0; i < 4; ++i) {
      const u32x4 fragv = {lo[i][0], lo[i][1], hi[i][0], hi[i][1]};
      size_t off;
      if (ISW1) {
        const int Tn = (job / 6) * 4 + i, s = (job % 6) * 3 + u;
        off = W1T_OFF + ((size_t)(Tn / 36) * 648 + frag_index1(Tn % 36, s)) * 1024;
        acc[i] = mfma16(fragv, bv[u], acc[i]);
      } else {
        const int un = 3 * job + u;
        if (un < 1296) {
          const int sg = un % 144;
          off = W2T_OFF + ((size_t)(sg / 18) * 648 + frag_index((un / 144) * 4 + i, sg % 18)) * 1024;
        } else {
          const int v = un - 1296;
          off = WOT_OFF + ((size_t)((v / 18) * 4 + i) * 18 + v % 18) * 1024;
        }
      }
      bst16<AUX_RT>(rs, lane * 16, (unsigned)off, fragv);
    }
    if (u == 0 && !ISW1) __builtin_amdgcn_s_setprio(0);
  }
  if (ISW1 && c < 2) {
#pragma unroll
    for (int i = 0; i < 4; ++i)
      *(f32x4*)(ws + CSB_OFF + ((size_t)((job % 6) * 2 + c) * F + ((job / 6) * 4 + i) * 16 + 4 * g) * 4) = acc[i];
  }
}
__global__ void __launch_bounds__(768, 3) k2_attn(const u16* __restrict__ w1, const u16* __restrict__ w2, const u16* __restrict__ wo,
                                                  char* __restrict__ ws) {
  __shared__ __attribute__((aligned(16))) char lds[163840];
  const int tid = threadIdx.x, lane = tid & 63, w = __builtin_amdgcn_readfirstlane(tid >> 6);
  const int xcd = blockIdx.x & 7, idx = blockIdx.x >> 3, h = 4 * xcd + (idx & 3), qb = idx >> 2;
  TSDECL; TS(2, 0);
  if (w >= 8) {
    __builtin_amdgcn_s_setprio(HELP_PRIO);
    {
      const char* src = ws + KV_OFF + (size_t)h * KVH + (w - 8) * 2048 + lane * 16;
      const unsigned lb = (unsigned)(size_t)(LAS char*)lds + (w - 8) * 2048;
      asm volatile("s_barrier" ::: "memory");
#pragma unroll
      for (int r = 4; r < 12; ++r) { glds16(src + r * 8192, lb + r * 8192); glds16(src + r * 8192 + 1024, lb + r * 8192 + 1024); }
      asm volatile("s_waitcnt vmcnt(12)\n\ts_barrier" ::: "memory");
#pragma unroll
      for (int r = 12; r < 19; ++r) { glds16(src + r * 8192, lb + r * 8192); glds16(src + r * 8192 + 1024, lb + r * 8192 + 1024); }
      asm volatile("s_waitcnt vmcnt(0)\n\ts_barrier" ::: "memory");
    }
    TS(2, 1);
    LAS char* stage = (LAS char*)lds + KVH + (w - 8) * 2048;
    const int job = blockIdx.x * 2 + (w & 1);
    if (w < 10) { if (job < 432) retile_units<3, true>(w1, w2, wo, ws, stage, job, lane); }
    else if (job < 486) retile_units<3, false>(w1, w2, wo, ws, stage, job, lane);
    TS(2, 2); TSEND(2);
    return;
  }
  const int qt = qb * 8 + w;
  u32x4 q0 = *(const u32x4*)(ws + QLO_OFF + ((size_t)h * 64 + qt) * 1024 + lane * 16);
  unsigned qh = *(const unsigned*)(ws + QHI_OFF + ((size_t)h * 2048 + qt * 32 + (lane & 31)) * 4);
  const char* src = ws + KV_OFF + (size_t)h * KVH + w * 1024 + lane * 16;
  const unsigned lb = (unsigned)(size_t)(LAS char*)lds + w * 1024;
#pragma unroll
  for (int r = 0; r < 4; ++r) glds16(src + r * 8192, lb + r * 8192);
  asm volatile("" : "+v"(q0), "+v"(qh));
  WAIT_VM(0);
  const u32x4 q1 = {lane < 32 ? qh : 0u, 0u, 0u, 0u};
  const LAS char* kv = (const LAS char*)lds;
  f32x16 o;
#pragma unroll
  for (int i = 0; i < 16; ++i) o[i] = 0.f;
  asm volatile("s_barrier" ::: "memory");
  TS(2, 1);
  attn_all<false, true>(kv, q0, q1, 0.f, lane, o);
  TS(2, 5);
  float l = __shfl(o[10], lane & 31);
  bool bad = !(l >= 7.8886091e-31f && l <= 1.3292280e36f);
#pragma unroll
  for (int i = 0; i < 10; ++i) bad |= !(__builtin_fabsf(o[i]) <= 3.0e38f) && (i < 8 || lane < 32);
  if (__builtin_expect(__any(bad), 0) && ATT_VAR < 3) {
    float m = -3.0e38f;
    const LAS char* kp = kv + lane * 16;
    const LAS char* hp = kv + 1024 + (lane & 31) * 4;
    f32x16 z;
#pragma unroll
    for (int i = 0; i < 16; ++i) z[i] = 0.f;
#pragma unroll 1
    for (int t = 0; t < 64; ++t) {
      const u32x4 ka = lds128(kp + t * KVT);
      const u32x4 kb = {*(const LAS unsigned*)(hp + t * KVT), 0u, 0u, 0u};
      f32x16 sc = mfma32(ka, q0, z);
      sc = mfma32(kb, q1, sc);
#pragma unroll
      for (int i = 0; i < 16; ++i) m = __builtin_fmaxf(m, sc[i]);
    }
    m = __builtin_fmaxf(m, __shfl_xor(m, 32));
#pragma unroll
    for (int i = 0; i < 16; ++i) o[i] = 0.f;
    attn_all<true, false>(kv, q0, q1, -m, lane, o);
    l = __shfl(o[10], lane & 31);
  }
  const float rl = 1.f / l;
  const int hf = lane >> 5, ci = 9 * (h >> 2) + 2 * (h & 3) + hf, ch = 9 * (h >> 2) + 8;
  const unsigned tokoff = ((lane >> 4) & 1) * 1024 + (lane & 15) * 16, ob = (unsigned)OF_OFF + qt * 36864;
  unsigned a0 = cvtpk(o[0] * rl, o[1] * rl), a1 = cvtpk(o[2] * rl, o[3] * rl), b0 = cvtpk(o[4] * rl, o[5] * rl), b1 = cvtpk(o[6] * rl, o[7] * rl);
  swap32(a0, b0); swap32(a1, b1);
  const rsrc_t rs = ws_rsrc(ws);
  bst16<AUX_O>(rs, (ci >> 2) * 2048 + (ci & 3) * 256 + tokoff, ob, u32x4{a0, a1, b0, b1});
  if (lane < 32) bst4<0>(rs, (ch >> 2) * 2048 + (ch & 3) * 256 + tokoff + (h & 3) * 4, ob, cvtpk(o[8] * rl, o[9] * rl));
  TS(2, 6); TSEND(2);
}

#define K4_X 0
#define K4_WIN 8
struct Silu { float z[4], e[4]; unsigned p[2]; };
constexpr int SOPS = 27;
#define PIN(x) asm volatile("" : "+v"(x))
template <int k>
DI void silu_op(Silu& q, const f32x4& v, const f32x4& cs, const f32x4& bs, float rs, float nm, LAS char* dst) {
  if constexpr (k < 4) { q.z[k] = __builtin_fmaf(cs[k], nm, bs[k]); PIN(q.z[k]); }
  else if constexpr (k < 8) { q.z[k - 4] = __builtin_fmaf(v[k - 4], rs, q.z[k - 4]); PIN(q.z[k - 4]); }
  else if constexpr (k < 12) { q.e[k - 8] = __builtin_amdgcn_exp2f(q.z[k - 8]); PIN(q.e[k - 8]); }
  else if constexpr (k < 16) { q.e[k - 12] = 1.f + q.e[k - 12]; PIN(q.e[k - 12]); }
  else if constexpr (k < 20) { q.e[k - 16] = __builtin_amdgcn_rcpf(q.e[k - 16]); PIN(q.e[k - 16]); }
  else if constexpr (k < 24) { q.e[k - 20] = q.z[k - 20] * q.e[k - 20]; PIN(q.e[k - 20]); }
  else if constexpr (k < 26) { q.p[k - 24] = cvtpk(q.e[2 * (k - 24)], q.e[2 * (k - 24) + 1]); PIN(q.p[k - 24]); }
  else *(LAS u32x2*)dst = u32x2{q.p[0], q.p[1]};
}
DI u32x4 bld16s(rsrc_t r, unsigned voff, unsigned soff) { return __builtin_amdgcn_raw_buffer_load_b128(r, voff, soff, 16); }
DI void wait_count(GAS unsigned* cnt) {
  for (unsigned spins = 0; spins < (1u << 20); ++spins) {
    if (__hip_atomic_load(cnt, __ATOMIC_RELAXED, __HIP_MEMORY_SCOPE_AGENT) >= 8u) break;
    __builtin_amdgcn_s_sleep(1);
  }
}
#define K4_W0 14
#define K4_SH0 16
constexpr int wdepth(int i) { return i < K4_SH0 ? K4_W0 : (K4_W0 - (i - K4_SH0) / 2 > K4_WIN ? K4_W0 - (i - K4_SH0) / 2 : K4_WIN); }
template <int NT>
DI void mlp_body(char* __restrict__ ws, u16* __restrict__ out, LAS char* hb, LAS char* h1, LAS float* st, LAS float* csb, LAS char* xk, int w, int lane, int fr, int rb PROFARG) {
  constexpr int NB = NT - 2, NF = NT * KS, WIN = K4_WIN;
  const int T0 = NT == 5 ? 5 * w : 20 + 4 * (w - 4);
  const int TA = 2 * w, TB = NT == 5 ? 16 + 3 * w : 28 + 2 * (w - 4);
  const unsigned wbase = (unsigned)(fr * 648 + (NT == 5 ? w * 90 : 360 + (w - 4) * 72)) * 1024u;
  const rsrc_t rs_ = ws_rsrc(ws);
  const unsigned vo = lane * 16;
  const unsigned o1 = (unsigned)W1T_OFF + wbase, o2 = (unsigned)W2T_OFF + wbase;
#define K4_EXP 0
#define WLOAD(i) bld16(rs_, vo, (i) < NF ? o1 + (i) * 1024u : o2 + ((i) - NF) * 1024u)
  u32x4 fr_[2 * NF];
#define FRONT(i) ((i) + 1 + wdepth(i) < 2 * NF ? (i) + 1 + wdepth(i) : 2 * NF)
#define REFILL(i) sfor<FRONT((i) - 1), FRONT(i)>(LAM(k_) { fr_[k_.v] = WLOAD(k_.v); })
  sfor<0, FRONT(-1)>(LAM(k_) { fr_[k_.v] = WLOAD(k_.v); });
  SBAR();
  constexpr float NL2E = -1.4426950408889634f;
  if (NT == 5 && w == 0) wait_count((GAS unsigned*)(ws + CNT_OFF + rb * 128));
  asm volatile("s_barrier" ::: "memory");
  TS(4, 13);
  {
    u32x4 xg[9];
#pragma unroll
    for (int i = 0; i < 9; ++i) xg[i] = bld16s(rs_, vo, (unsigned)X1G_OFF + rb * 73728 + (w * 9 + i) * 1024);
    if (NT == 5 && w == 0) {
      f32x4 sv[4];
#pragma unroll
      for (int i = 0; i < 4; ++i)
        sv[i] = __builtin_bit_cast(f32x4, bld16s(rs_, lane * 32 + (i & 1) * 16, (unsigned)STAT_OFF + (rb * 2 + (i >> 1)) * 2048));
      const f32x4 t1 = sv[0] + sv[1], t2 = sv[2] + sv[3];
      const float s1 = (t1[0] + t1[1]) + (t1[2] + t1[3]), s2 = (t2[0] + t2[1]) + (t2[2] + t2[3]);
      const float mean = s1 * (1.f / D);
      const float var = __builtin_fmaxf(s2 * (1.f / D) - mean * mean, 0.f);
      const float rstd = __builtin_amdgcn_rsqf(var + 1e-5f);
      st[2 * lane] = rstd * NL2E; st[2 * lane + 1] = -mean * rstd;
    }
#pragma unroll
    for (int i = 0; i < 9; ++i) *(LAS u32x4*)(hb + (w * 9 + i) * 1024 + lane * 16) = xg[i];
  }
  LDSBAR();
  TS(4, 2);
  unsigned bp0 = (unsigned)(size_t)hb + lane * 16, bp1 = bp0 + 40960, hp0 = (unsigned)(size_t)h1 + lane * 16, hp1 = hp0 + 40960;
  asm volatile("" : "+v"(bp0), "+v"(bp1), "+v"(hp0), "+v"(hp1));
#define XFRAG(f) lds128((const LAS char*)(size_t)((f) < 40 ? bp0 + (f) * 1024 : bp1 + ((f) - 40) * 1024))
#define HFRAG(f) lds128((const LAS char*)(size_t)((f) < 40 ? hp0 + (f) * 1024 : hp1 + ((f) - 40) * 1024))
  const int g = lane >> 4, c = lane & 15;
  f32x4 accA[2][4], accB[NB][4], acc[NT][4];
  u32x4 bf[2][4];
  const f32x4 z4 = {0.f, 0.f, 0.f, 0.f};
#pragma unroll
  for (int t = 0; t < 4; ++t) bf[0][t] = XFRAG(t);
  sfor<0, KS>(LAM(s_) {
    constexpr int s = s_.v;
#pragma unroll
    for (int t = 0; t < 4; ++t) bf[(s + 1) & 1][t] = XFRAG(((s + 1) % KS) * 4 + t);
    SBAR();
    sfor<0, 2>(LAM(j_) {
      constexpr int j = j_.v, i = s * 2 + j;
#pragma unroll
      for (int t = 0; t < 4; ++t) { accA[j][t] = mfma16(fr_[i], bf[s & 1][t], s == 0 ? z4 : accA[j][t]); PIN(accA[j][t]); }
      SBAR();
      REFILL(i);
      SBAR();
    });
  });
  TS(4, 3);
  float rs[4], nm[4];
#pragma unroll
  for (int t = 0; t < 4; ++t) { rs[t] = st[2 * (t * 16 + c)]; nm[t] = st[2 * (t * 16 + c) + 1]; }
  const LAS float* csg = csb + 4 * g;
  f32x4 cs[2], bs[2];
  cs[0] = *(const LAS f32x4*)(csg + TA * 16); bs[0] = *(const LAS f32x4*)(csg + 576 + TA * 16);
  cs[1] = *(const LAS f32x4*)(csg + TA * 16 + 16); bs[1] = *(const LAS f32x4*)(csg + 576 + TA * 16 + 16);
  Silu sq;
  sfor<0, KS>(LAM(s_) {
    constexpr int s = s_.v, M = 16 * NB * 4, TOT = 8 * SOPS;
    if constexpr (s + 1 < KS) {
#pragma unroll
      for (int t = 0; t < 4; ++t) bf[(s + 1) & 1][t] = XFRAG((s + 1) * 4 + t);
    }
    SBAR();
    sfor<0, NB>(LAM(j_) {
      constexpr int j = j_.v, i = 36 + s * NB + j;
      sfor<0, 4>(LAM(t_) {
        constexpr int t = t_.v, m = (s * NB + j) * 4 + t;
        accB[j][t] = mfma16(fr_[i], bf[s & 1][t], s == 0 ? z4 : accB[j][t]);
        PIN(accB[j][t]);
        if constexpr (m < M) {
          sfor<m * TOT / M, (m + 1) * TOT / M>(LAM(o_) {
            constexpr int o = o_.v, q = o / SOPS, jj = q >> 2, tt = q & 3;
            silu_op<o % SOPS>(sq, accA[jj][tt], cs[jj], bs[jj], rs[tt], nm[tt], h1 + (w * 4 + tt) * 1024 + lane * 16 + jj * 8);
          });
        }
        SBAR();
      });
      REFILL(i);
      SBAR();
    });
  });
  cs[0] = *(const LAS f32x4*)(csg + TB * 16); bs[0] = *(const LAS f32x4*)(csg + 576 + TB * 16);
  cs[1] = *(const LAS f32x4*)(csg + TB * 16 + 16); bs[1] = *(const LAS f32x4*)(csg + 576 + TB * 16 + 16);
  asm volatile("s_waitcnt lgkmcnt(0)\n\ts_barrier" ::: "memory");
  TS(4, 5);
#pragma unroll
  for (int t = 0; t < 4; ++t) bf[0][t] = HFRAG(t);
  sfor<0, 8>(LAM(s_) {
    constexpr int s = s_.v, M = 8 * NT * 4, TOT = NB * 4 * SOPS;
    if constexpr (s + 1 < 8) {
#pragma unroll
      for (int t = 0; t < 4; ++t) bf[(s + 1) & 1][t] = HFRAG((s + 1) * 4 + t);
    }
    SBAR();
    sfor<0, NT>(LAM(j_) {
      constexpr int j = j_.v, i = NF + s * NT + j;
      sfor<0, 4>(LAM(t_) {
        constexpr int t = t_.v, m = (s * NT + j) * 4 + t;
        acc[j][t] = mfma16(fr_[i], bf[s & 1][t], s == 0 ? z4 : acc[j][t]);
        PIN(acc[j][t]);
        sfor<m * TOT / M, (m + 1) * TOT / M>(LAM(o_) {
          constexpr int o = o_.v, q = o / SOPS, jj = q >> 2, tt = q & 3;
          const int T = TB + jj;
          if constexpr (NB == 3 && o == 4 * SOPS + 8) {
            cs[0] = *(const LAS f32x4*)(csg + TB * 16 + 32); bs[0] = *(const LAS f32x4*)(csg + 576 + TB * 16 + 32);
          }
          silu_op<o % SOPS>(sq, accB[jj][tt], cs[jj & 1], bs[jj & 1], rs[tt], nm[tt], h1 + ((T >> 1) * 4 + tt) * 1024 + lane * 16 + (T & 1) * 8);
        });
        SBAR();
      });
      REFILL(i);
      SBAR();
    });
  });
  asm volatile("s_waitcnt lgkmcnt(0)\n\ts_barrier" ::: "memory");
  TS(4, 7);
#pragma unroll
  for (int t = 0; t < 4; ++t) bf[0][t] = HFRAG(32 + t);
  sfor<8, KS>(LAM(s_) {
    constexpr int s = s_.v;
    if constexpr (s + 1 < KS) {
#pragma unroll
      for (int t = 0; t < 4; ++t) bf[(s + 1) & 1][t] = HFRAG((s + 1) * 4 + t);
    }
    SBAR();
    sfor<0, NT>(LAM(j_) {
      constexpr int j = j_.v, i = NF + s * NT + j;
#pragma unroll
      for (int t = 0; t < 4; ++t) { acc[j][t] = mfma16(fr_[i], bf[s & 1][t], acc[j][t]); PIN(acc[j][t]); }
      SBAR();
      REFILL(i);
      SBAR();
    });
  });
  TS(4, 8);
#pragma unroll
  for (int j = 0; j < NT; ++j) {
    const int T = T0 + j;
#pragma unroll
    for (int tp = 0; tp < 2; ++tp) {
      const f32x4 v0 = acc[j][2 * tp], v1 = acc[j][2 * tp + 1];
      u32x4 o = {cvtpk(v0[0], v0[1]), cvtpk(v0[2], v0[3]), cvtpk(v1[0], v1[1]), cvtpk(v1[2], v1[3])};
      const int ul = T * 2 + tp - fr * 9;
      if (ul >= 0 && ul < 9) *(LAS u32x4*)(hb + ul * 1024 + vo) = o;
      else bst16<AUX_PART>(rs_, vo, (unsigned)(PART_OFF + ((((size_t)fr * 32 + rb) * 36 + T) * 2 + tp) * 1024), o);
    }
  }
  TS(4, 9);
  const int u0 = fr * 9 + w;
  f32x4 gb = *(const f32x4*)(ws + MODF_OFF + (5 * D + (u0 >> 1) * 16 + 4 * g) * 4), gb2;
  if (w == 0) gb2 = *(const f32x4*)(ws + MODF_OFF + (5 * D + ((u0 + 8) >> 1) * 16 + 4 * g) * 4);
  asm volatile("s_waitcnt vmcnt(0) lgkmcnt(0)\n\ts_barrier" ::: "memory");
  TS(4, 10);
  if (NT == 5 && w == 0) {
    GAS unsigned* cnt = (GAS unsigned*)(ws + CNT_OFF + (32 + rb) * 128);
    if (lane == 0) __hip_atomic_fetch_add(cnt, 1u, __ATOMIC_RELAXED, __HIP_MEMORY_SCOPE_AGENT);
    wait_count(cnt);
  }
  asm volatile("s_barrier" ::: "memory");
  TS(4, 11);
  auto request = [&](int ul, u32x4 (&p)[7]) __attribute__((always_inline)) {
#pragma unroll
    for (int i = 0; i < 7; ++i) p[i] = bld16s(rs_, vo, (unsigned)PART_OFF + ((((fr + 1 + i) & 7) * 32 + rb) * 72 + fr * 9 + ul) * 1024);
  };
  auto reduce = [&](int ul, const u32x4 (&p)[7], const f32x4& gv) __attribute__((always_inline)) {
    const u32x4 own = lds128(hb + ul * 1024 + vo), xv = lds128(xk + ul * 1024 + vo);
    const f32x4 gm = gv * -0.6931471805599453f;
    const int unit = fr * 9 + ul, col = (unit >> 1) * 16 + 4 * g;
#pragma unroll
    for (int t = 0; t < 2; ++t) {
      float a[4] = {bflo(own[2 * t]), bfhi(own[2 * t]), bflo(own[2 * t + 1]), bfhi(own[2 * t + 1])};
#pragma unroll
      for (int f = 0; f < 7; ++f) {
        a[0] += bflo(p[f][2 * t]); a[1] += bfhi(p[f][2 * t]);
        a[2] += bflo(p[f][2 * t + 1]); a[3] += bfhi(p[f][2 * t + 1]);
      }
      u32x2 o = {cvtpk(bflo(xv[2 * t]) + gm[0] * a[0], bfhi(xv[2 * t]) + gm[1] * a[1]),
                 cvtpk(bflo(xv[2 * t + 1]) + gm[2] * a[2], bfhi(xv[2 * t + 1]) + gm[3] * a[3])};
      *(u32x2*)(out + (size_t)(rb * 64 + (unit & 1) * 32 + t * 16 + c) * D + col) = o;
    }
  };
  u32x4 p[7];
  request(w, p);
  if (NT == 5 && w == 0) {
    u32x4 p2[7];
    request(8, p2);
    SBAR();
    reduce(0, p, gb);
    reduce(8, p2, gb2);
  } else {
    reduce(w, p, gb);
  }
  TS(4, 12); TSEND(4);
#undef WLOAD
#undef FRONT
#undef REFILL
#undef XFRAG
#undef HFRAG
}
#define K4_MAP 2
__global__ void __launch_bounds__(512, 2) k4_mlp(const u16* __restrict__ x, char* __restrict__ ws, u16* __restrict__ out) {
  __shared__ __attribute__((aligned(16))) char lds[2 * 73728 + 512 + 4608 + 9216];
  const int tid = threadIdx.x, lane = tid & 63, w = __builtin_amdgcn_readfirstlane(tid >> 6);
  const int xcd = blockIdx.x & 7, idx = blockIdx.x >> 3;
  const int rb = (xcd >> 1) * 8 + (idx & 7), fr = (xcd & 1) * 4 + (idx >> 3);
  LAS char* hb = (LAS char*)lds;
  LAS char* h1 = hb + 73728;
  LAS float* st = (LAS float*)(hb + 2 * 73728);
  LAS float* csb = st + 128;
  LAS char* xk = (LAS char*)(csb + 1152);
  LAS float* red = (LAS float*)hb;
  TSDECL; TS(4, 0);
  const rsrc_t rs = ws_rsrc(ws);
  const unsigned vo = lane * 16;
  const int g = lane >> 4, c = lane & 15;
  {
    const char* src = ws + OF_OFF + (size_t)rb * 73728 + w * 9216 + lane * 16;
    const unsigned lb = (unsigned)(size_t)h1 + w * 9216;
#pragma unroll
    for (int i = 0; i < 9; ++i) glds16(src + i * 1024, lb + i * 1024);
  }
  if (w < 5) {
    const int Tf = (9 * fr + 1) >> 1, T = w < 4 ? Tf + w : (fr & 1) ? Tf - 1 : Tf + 4;
    constexpr int W3 = 10;
    const unsigned ao = (unsigned)WOT_OFF + T * 18 * 1024;
    u32x4 a[18];
    SBAR();
#pragma unroll
    for (int s = 0; s < W3; ++s) a[s] = bld16(rs, vo, ao + s * 1024);
    SBAR();
    asm volatile("s_waitcnt vmcnt(%0)\n\ts_barrier" :: "i"(W3) : "memory");
    TS(4, 1);
    u32x2 xr[4];
    f32x4 ga, gb2;
    f32x4 acc[4];
#pragma unroll
    for (int t = 0; t < 4; ++t) acc[t] = f32x4{0.f, 0.f, 0.f, 0.f};
    const LAS char* bp = h1 + vo;
    u32x4 b[2][4];
#pragma unroll
    for (int t = 0; t < 4; ++t) b[0][t] = lds128(bp + (t >> 1) * 36864 + (t & 1) * 1024);
#pragma unroll
    for (int s = 0; s < 18; ++s) {
      if (s + W3 < 18) a[s + W3] = bld16(rs, vo, ao + (s + W3) * 1024);
      if (s >= 8 && s < 12) xr[s - 8] = *(const u32x2*)(x + (size_t)(rb * 64 + (s - 8) * 16 + c) * D + T * 16 + 4 * g);
      if (s == 12) ga = *(const f32x4*)(ws + MODF_OFF + (2 * D + T * 16 + 4 * g) * 4);
      if (s == 13) gb2 = *(const f32x4*)(ws + MODF_OFF + (4 * D + T * 16 + 4 * g) * 4);
      if (s + 1 < 18) {
#pragma unroll
        for (int t = 0; t < 4; ++t) b[(s + 1) & 1][t] = lds128(bp + (t >> 1) * 36864 + ((s + 1) * 2 + (t & 1)) * 1024);
      }
      SBAR();
#pragma unroll
      for (int t = 0; t < 4; ++t) acc[t] = mfma16(a[s], b[s & 1][t], acc[t]);
      SBAR();
    }
    TS(4, 4);
#pragma unroll
    for (int tp = 0; tp < 2; ++tp) {
      const bool own = w < 4 || tp == (fr & 1);
      u32x4 xo;
      unsigned og[2][2];
#pragma unroll
      for (int t = 0; t < 2; ++t) {
        const int tt = 2 * tp + t;
        const unsigned o0 = cvtpk(bflo(xr[tt][0]) + ga[0] * acc[tt][0], bfhi(xr[tt][0]) + ga[1] * acc[tt][1]);
        const unsigned o1 = cvtpk(bflo(xr[tt][1]) + ga[2] * acc[tt][2], bfhi(xr[tt][1]) + ga[3] * acc[tt][3]);
        xo[2 * t] = o0; xo[2 * t + 1] = o1;
        const float v0 = bflo(o0), v1 = bfhi(o0), v2 = bflo(o1), v3 = bfhi(o1);
        og[t][0] = cvtpk(v0 * gb2[0], v1 * gb2[1]); og[t][1] = cvtpk(v2 * gb2[2], v3 * gb2[3]);
        const float s1 = sum_rows((v0 + v1) + (v2 + v3)), s2 = sum_rows((v0 * v0 + v1 * v1) + (v2 * v2 + v3 * v3));
        if (g == 0) { red[(w * 2) * 64 + tt * 16 + c] = own ? s1 : 0.f; red[(w * 2 + 1) * 64 + tt * 16 + c] = own ? s2 : 0.f; }
      }
      swap16(og[0][0], og[1][0]); swap16(og[0][1], og[1][1]);
      if (own) {
        *(LAS u32x4*)(xk + (2 * T + tp - 9 * fr) * 1024 + vo) = xo;
        bst16<16>(rs, (g & 1) * 1024 + ((2 * (T & 1) + (g >> 1)) * 16 + c) * 16,
                  (unsigned)X1G_OFF + rb * 73728 + ((T >> 1) * 4 + tp * 2) * 1024, u32x4{og[0][0], og[0][1], og[1][0], og[1][1]});
      }
    }
  } else {
    asm volatile("s_waitcnt vmcnt(0)\n\ts_barrier" ::: "memory");
    const int i0 = (w - 5) * 64 + lane, i1 = i0 + 192;
    const unsigned v0 = ((i0 >= 144) * F + fr * 576 + 4 * (i0 >= 144 ? i0 - 144 : i0)) * 4;
    const unsigned v1 = i1 < 288 ? (F + fr * 576 + 4 * (i1 - 144)) * 4 : OOB;
    u32x4 c0[6], c1[6];
#pragma unroll
    for (int k = 0; k < 6; ++k) { c0[k] = bld16(rs, v0, (unsigned)CSB_OFF + k * 2 * F * 4); c1[k] = bld16(rs, v1, (unsigned)CSB_OFF + k * 2 * F * 4); }
    f32x4 s0 = __builtin_bit_cast(f32x4, c0[0]), s1 = __builtin_bit_cast(f32x4, c1[0]);
#pragma unroll
    for (int k = 1; k < 6; ++k) { s0 += __builtin_bit_cast(f32x4, c0[k]); s1 += __builtin_bit_cast(f32x4, c1[k]); }
    *(LAS f32x4*)(csb + 4 * i0) = s0 * -1.4426950408889634f;
    if (i1 < 288) *(LAS f32x4*)(csb + 4 * i1) = s1 * -1.4426950408889634f;
  }
  LDSBAR();
  if (tid < 128) {
    float sm = 0.f;
#pragma unroll
    for (int i = 0; i < 5; ++i) sm += red[(i * 2 + (tid >> 6)) * 64 + (tid & 63)];
    bst4<16>(rs, ((rb * 2 + (tid >> 6)) * 64 + (tid & 63)) * 32 + fr * 4, (unsigned)STAT_OFF, __builtin_bit_cast(unsigned, sm));
  }
  asm volatile("s_waitcnt vmcnt(0)\n\ts_barrier" ::: "memory");
  if (tid == 0) __hip_atomic_fetch_add((GAS unsigned*)(ws + CNT_OFF + rb * 128), 1u, __ATOMIC_RELAXED, __HIP_MEMORY_SCOPE_AGENT);
  TS(4, 6);
  if (w < 4) mlp_body<5>(ws, out, hb, h1, st, csb, xk, w, lane, fr, rb PROFPASS);
  else mlp_body<4>(ws, out, hb, h1, st, csb, xk, w, lane, fr, rb PROFPASS);
}

extern "C" void kernel_launch(void* const* d_in, const int* in_sizes, int n_in, void* d_out, int out_size, void* d_ws,
                              size_t ws_size, hipStream_t stream) {
  const u16* x = (const u16*)d_in[0];
  const u16* wqkv = (const u16*)d_in[1];
  const u16* wo = (const u16*)d_in[2];
  const u16* w1 = (const u16*)d_in[4];
  const u16* w2 = (const u16*)d_in[5];
  const u16* c = (const u16*)d_in[6];
  const u16* wmod = (const u16*)d_in[7];
  char* ws = (char*)d_ws;
  k0_prep<<<K0_MOD + K0_LN + K0_QKV_BLKS, 256, 0, stream>>>(c, wmod, wqkv, x, ws);
  k1_qkv<<<256, 512, 0, stream>>>(ws);
  k2_attn<<<256, 768, 0, stream>>>(w1, w2, wo, ws);
  k4_mlp<<<256, 512, 0, stream>>>(x, ws, (u16*)d_out);
}
```

```cpp
#include <hip/hip_runtime.h>
#include <hip/hip_bf16.h>
#include <stdint.h>

typedef unsigned short u16;
typedef short bf16x8 __attribute__((ext_vector_type(8)));
typedef short s16x4 __attribute__((ext_vector_type(4)));
typedef float f32x4 __attribute__((ext_vector_type(4)));
typedef float f32x16 __attribute__((ext_vector_type(16)));
typedef unsigned u32x4 __attribute__((ext_vector_type(4)));
typedef unsigned u32x2 __attribute__((ext_vector_type(2)));
#define LAS __attribute__((address_space(3)))
#define GAS __attribute__((address_space(1)))
#define DI __device__ __forceinline__

constexpr int S = 2048, D = 576, NH = 32, DH = 18, F = 4608;
constexpr int KS = 18;

constexpr size_t MODP_OFF = 0;
constexpr size_t WQKVT_OFF = 0x10000;
constexpr size_t WOT_OFF = WQKVT_OFF + 8 * 14 * 18 * 1024;
constexpr size_t W1T_OFF = WOT_OFF + 36 * 18 * 1024;
constexpr size_t W2T_OFF = W1T_OFF + 8 * 648 * 1024;
constexpr size_t QLO_OFF = W2T_OFF + 8 * 648 * 1024;
constexpr size_t QHI_OFF = QLO_OFF + 32 * 64 * 1024;
constexpr int KVT = 2432;
constexpr int KVH = 64 * KVT;
constexpr size_t KV_OFF = QHI_OFF + 32 * 2048 * 4;
constexpr size_t OF_OFF = KV_OFF + (size_t)32 * KVH;
constexpr size_t X1_OFF = OF_OFF + (size_t)S * D * 2;
constexpr size_t X1G_OFF = X1_OFF + (size_t)S * D * 2;
constexpr size_t NF_OFF = X1G_OFF + (size_t)S * D * 2;
constexpr size_t STAT_OFF = NF_OFF + (size_t)S * D * 2;
 constexpr size_t CSB_OFF = STAT_OFF + 32 * 2 * 64 * 8 * 4;
constexpr size_t CSBF_OFF = CSB_OFF + 6 * 2 * 4608 * 4;
constexpr size_t MODF_OFF = CSBF_OFF + 2 * 4608 * 4;
constexpr size_t CNT_OFF = MODF_OFF + 6 * D * 4;
constexpr size_t PART_OFF = CSB_OFF + 0x50000;
constexpr size_t WS_END = PART_OFF + (size_t)8 * S * D * 2;

#define TSDECL
#define TS(kid, i)
#define TSEND(kid)
#define PROFARG
#define PROFPASS
template <int I> struct IC { static constexpr int v = I; };
template <int B, int E, class Fn> __device__ __forceinline__ void sfor(Fn&& f) {
  if constexpr (B < E) { f(IC<B>{}); sfor<B + 1, E>(f); }
}
#define LAM(x) [&](auto x) __attribute__((always_inline))
DI float bf2f(unsigned v) { return __builtin_bit_cast(float, v << 16); }
DI float bflo(unsigned v) { return __builtin_bit_cast(float, v << 16); }
DI float bfhi(unsigned v) { return __builtin_bit_cast(float, v & 0xffff0000u); }
typedef __bf16 bf16x2_t __attribute__((ext_vector_type(2)));
typedef float f32x2 __attribute__((ext_vector_type(2)));
DI unsigned cvtpk(float lo, float hi) {
  return __builtin_bit_cast(unsigned, __builtin_convertvector(f32x2{lo, hi}, bf16x2_t));
}
DI float rbf(float x) { return bflo(cvtpk(x, x)); }
DI f32x4 mfma16(u32x4 a, u32x4 b, f32x4 c) {
  return __builtin_amdgcn_mfma_f32_16x16x32_bf16(__builtin_bit_cast(bf16x8, a), __builtin_bit_cast(bf16x8, b), c, 0, 0, 0);
}
DI f32x16 mfma32(u32x4 a, u32x4 b, f32x16 c) {
  return __builtin_amdgcn_mfma_f32_32x32x16_bf16(__builtin_bit_cast(bf16x8, a), __builtin_bit_cast(bf16x8, b), c, 0, 0, 0);
}
DI void glds16(const void* g, unsigned lds_base) {
  unsigned sv;
  asm volatile("s_mov_b32 %0, m0\n\ts_mov_b32 m0, %2\n\ts_nop 0\n\tglobal_load_lds_dwordx4 %1, off\n\ts_mov_b32 m0, %0"
               : "=&s"(sv) : "v"(g), "s"(lds_base) : "memory");
}
typedef __amdgpu_buffer_rsrc_t rsrc_t;
DI rsrc_t ws_rsrc(const char* ws) { return __builtin_amdgcn_make_buffer_rsrc((void*)ws, (short)0, (int)WS_END, 0x00020000); }
template <int AUX> DI void bst16(rsrc_t r, unsigned voff, unsigned soff, u32x4 v) { __builtin_amdgcn_raw_buffer_store_b128(v, r, voff + soff, 0, AUX); }
template <int AUX> DI void bst8(rsrc_t r, unsigned voff, unsigned soff, u32x2 v) { __builtin_amdgcn_raw_buffer_store_b64(v, r, voff, soff, AUX); }
template <int AUX> DI void bst4(rsrc_t r, unsigned voff, unsigned soff, unsigned v) { __builtin_amdgcn_raw_buffer_store_b32(v, r, voff, soff, AUX); }
DI u32x4 bld16(rsrc_t r, unsigned voff, unsigned soff) { return __builtin_amdgcn_raw_buffer_load_b128(r, voff, soff, 0); }
#define AUX_RT 16
#define AUX_K0L 16
#define AUX_K0G 16
#define AUX_O 16
#define AUX_PART 16
#define WAIT_VM(n) asm volatile("s_waitcnt vmcnt(" #n ")" ::: "memory")
#define SBAR() __builtin_amdgcn_sched_barrier(0)
DI float silu_f(float x) { return x * __builtin_amdgcn_rcpf(1.f + __builtin_amdgcn_exp2f(-1.4426950408889634f * x)); }
typedef short v4i16_t __attribute__((ext_vector_type(4)));
DI u32x2 tr_read(const LAS char* p) {
  return __builtin_bit_cast(u32x2, __builtin_amdgcn_ds_read_tr16_b64_v4i16((LAS v4i16_t*)p));
}
template <int CTRL> DI unsigned dppu(unsigned v) { return __builtin_amdgcn_update_dpp(0u, v, CTRL, 0xF, 0xF, true); }
DI unsigned dpp_xor1(unsigned v) { return dppu<0xB1>(v); }
template <int CTRL> DI float dppf(float v) { return __builtin_bit_cast(float, dppu<CTRL>(__builtin_bit_cast(unsigned, v))); }
DI float sum16(float v) { v += dppf<0xB1>(v); v += dppf<0x4E>(v); v += dppf<0x141>(v); v += dppf<0x140>(v); return v; }
DI u32x4 lds128(const LAS char* p) { return *(const LAS u32x4*)p; }

DI int frag_index(int T, int s) {
  return T < 20 ? (T / 5) * 90 + s * 5 + (T % 5) : 360 + ((T - 20) >> 2) * 72 + s * 4 + ((T - 20) & 3);
}

DI int frag_index1(int T, int s) {
  if (T < 16) { const int w = T >> 1; return (w < 4 ? 90 * w : 360 + 72 * (w - 4)) + 2 * s + (T & 1); }
  if (T < 28) { const int w = (T - 16) / 3; return 90 * w + 36 + 3 * s + (T - 16) % 3; }
  const int w = 4 + ((T - 28) >> 1);
  return 360 + 72 * (w - 4) + 36 + 2 * s + (T & 1);
}

constexpr int K0_MOD = 216, K0_LN = 128, K0_QKV_BLKS = 144;
__global__ void __launch_bounds__(256) k0_prep(const u16* __restrict__ c, const u16* __restrict__ wmod, const u16* __restrict__ wqkv,
                                               const u16* __restrict__ x, char* __restrict__ ws) {
  __shared__ __attribute__((aligned(16))) char lds[18432];
  const int tid = threadIdx.x, lane = tid & 63, w = __builtin_amdgcn_readfirstlane(tid >> 6);
  int b = blockIdx.x;
  TSDECL; TS(0, 0);
  if (b < K0_MOD) {
    const int cc = b % 54, ks = b / 54;
    const int ch = tid & 7, rg = tid >> 3;
    float acc[8];
#pragma unroll
    for (int j = 0; j < 8; ++j) acc[j] = 0.f;
    u32x4 wv[5];
    float sv[5];
    unsigned cr[5];
#pragma unroll
    for (int i = 0; i < 5; ++i) {
      const int r = rg + 32 * i, k = ks * 144 + (r < 144 ? r : 0);
      cr[i] = c[k];
      wv[i] = *(const u32x4*)(wmod + (size_t)k * 3456 + cc * 64 + ch * 8);
    }
    SBAR();
#pragma unroll
    for (int i = 0; i < 5; ++i) sv[i] = rbf(silu_f(bf2f(cr[i]))) * (rg + 32 * i < 144 ? 1.f : 0.f);
#pragma unroll
    for (int i = 0; i < 5; ++i)
#pragma unroll
      for (int j = 0; j < 4; ++j) {
        acc[2 * j] += sv[i] * bflo(wv[i][j]);
        acc[2 * j + 1] += sv[i] * bfhi(wv[i][j]);
      }
#pragma unroll
    for (int j = 0; j < 8; ++j) {
      acc[j] += __shfl_xor(acc[j], 8);
      acc[j] += __shfl_xor(acc[j], 16);
      acc[j] += __shfl_xor(acc[j], 32);
    }
    float* red = (float*)lds;
    if (lane < 8)
#pragma unroll
      for (int j = 0; j < 8; ++j) red[w * 64 + lane * 8 + j] = acc[j];
    __syncthreads();
    if (tid < 64) ((float*)(ws + MODP_OFF))[ks * 3456 + cc * 64 + tid] = red[tid] + red[64 + tid] + red[128 + tid] + red[192 + tid];
    TS(0, 1); TSEND(0);
    return;
  }
  b -= K0_MOD;
  if (b < K0_LN) {
    const int tok = b * 16 + w * 4 + (lane >> 4), p = lane & 15;
    const u16* xp = x + (size_t)tok * D + 4 * p;
    u32x2 xv[9];
#pragma unroll
    for (int i = 0; i < 9; ++i) xv[i] = *(const u32x2*)(xp + 64 * i);
    float sm = 0.f;
#pragma unroll
    for (int i = 0; i < 9; ++i) sm += (bflo(xv[i][0]) + bfhi(xv[i][0])) + (bflo(xv[i][1]) + bfhi(xv[i][1]));
    const float mean = sum16(sm) * (1.f / D);
    float q = 0.f;
#pragma unroll
    for (int i = 0; i < 9; ++i)
#pragma unroll
      for (int j = 0; j < 2; ++j) { float a0 = bflo(xv[i][j]) - mean, a1 = bfhi(xv[i][j]) - mean; q += a0 * a0; q += a1 * a1; }
    const float rstd = __builtin_amdgcn_rsqf(sum16(q) * (1.f / D) + 1e-5f);
    LAS char* dst = (LAS char*)lds + (p >> 3) * 1024 + ((p >> 1) & 3) * 256 + (tok & 15) * 16 + (p & 1) * 8;
#pragma unroll
    for (int i = 0; i < 9; ++i) {
      u32x2 o = {cvtpk((bflo(xv[i][0]) - mean) * rstd, (bfhi(xv[i][0]) - mean) * rstd),
                 cvtpk((bflo(xv[i][1]) - mean) * rstd, (bfhi(xv[i][1]) - mean) * rstd)};
      *(LAS u32x2*)(dst + i * 2048) = o;
    }
    __syncthreads();
    const rsrc_t rs0 = ws_rsrc(ws);
#pragma unroll
    for (int i = 0; i < 5; ++i) {
      const int s = w + 4 * i;
      if (s < 18) bst16<AUX_K0L>(rs0, lane * 16, (unsigned)NF_OFF + (b >> 2) * 73728 + s * 4096 + (b & 3) * 1024, lds128((const LAS char*)lds + s * 1024 + lane * 16));
    }
    TS(0, 1); TSEND(0);
    return;
  }
  b -= K0_LN;
  {
    const int s = b % 18, hq = b / 18;
    constexpr int PITCH = 448;
    u32x4 raw[4];
#pragma unroll
    for (int i = 0; i < 4; ++i) {
      const int e = min(tid + 256 * i, 863), row = e / 27, pc = e % 27;
      raw[i] = *(const u32x4*)(wqkv + (size_t)(32 * s + row) * 1728 + (pc / 9) * 576 + hq * 72 + (pc % 9) * 8);
    }
#pragma unroll
    for (int i = 0; i < 4; ++i) {
      const int e = min(tid + 256 * i, 863), row = e / 27, pc = e % 27;
      *(LAS u32x4*)((LAS char*)lds + row * PITCH + pc * 16) = raw[i];
    }
    __syncthreads();
    const int g = lane >> 4, r = lane & 15;
    const rsrc_t rs0 = ws_rsrc(ws);
#pragma unroll
    for (int i = 0; i < 4; ++i) {
      const int tile = w + 4 * i;
      if (tile < 14) {
        int col; bool ok = true;
        if (tile < 12) col = (tile >> 2) * 72 + (tile & 3) * 18 + r;
        else if (tile == 12) col = (r >> 3) * 72 + ((r >> 1) & 3) * 18 + 16 + (r & 1);
        else { ok = r < 8; col = 144 + ((r >> 1) & 3) * 18 + 16 + (r & 1); }
        const LAS u16* p = (const LAS u16*)((LAS char*)lds + 8 * g * PITCH) + col;
        u32x4 o;
#pragma unroll
        for (int j = 0; j < 4; ++j) o[j] = ok ? ((unsigned)p[(2 * j) * (PITCH / 2)] | ((unsigned)p[(2 * j + 1) * (PITCH / 2)] << 16)) : 0u;
        bst16<AUX_K0G>(rs0, lane * 16, (unsigned)WQKVT_OFF + ((hq * 14 + tile) * 18 + s) * 1024, o);
      }
    }
    TS(0, 1); TSEND(0);
  }
}

DI f32x4 mod4(const float* __restrict__ modp, int col) {
  const f32x4 a = *(const f32x4*)(modp + col), b = *(const f32x4*)(modp + 3456 + col), c = *(const f32x4*)(modp + 2 * 3456 + col),
              d = *(const f32x4*)(modp + 3 * 3456 + col);
  return f32x4{rbf(a[0] + b[0] + c[0] + d[0]), rbf(a[1] + b[1] + c[1] + d[1]), rbf(a[2] + b[2] + c[2] + d[2]), rbf(a[3] + b[3] + c[3] + d[3])};
}
DI f32x4 gain4(f32x4 v) { return f32x4{rbf(1.f + v[0]), rbf(1.f + v[1]), rbf(1.f + v[2]), rbf(1.f + v[3])}; }

constexpr float QSCALE = 0.23570226039551584f * 1.4426950408889634f;
DI u32x4 modulate8(u32x4 v, const f32x4& g0, const f32x4& g1, const f32x4& s0, const f32x4& s1) {
  return u32x4{cvtpk(bflo(v[0]) * g0[0] + s0[0], bfhi(v[0]) * g0[1] + s0[1]), cvtpk(bflo(v[1]) * g0[2] + s0[2], bfhi(v[1]) * g0[3] + s0[3]),
               cvtpk(bflo(v[2]) * g1[0] + s1[0], bfhi(v[2]) * g1[1] + s1[1]), cvtpk(bflo(v[3]) * g1[2] + s1[2], bfhi(v[3]) * g1[3] + s1[3])};
}
#define AUX_K1 16
#define RAWBAR() asm volatile("s_barrier" ::: "memory")
#define LDSBAR() asm volatile("s_waitcnt lgkmcnt(0)\n\ts_barrier" ::: "memory")
DI void swap16(unsigned& a, unsigned& b) { const auto r = __builtin_amdgcn_permlane16_swap(a, b, false, false); a = r[0]; b = r[1]; }
DI void swap32(unsigned& a, unsigned& b) { const auto r = __builtin_amdgcn_permlane32_swap(a, b, false, false); a = r[0]; b = r[1]; }
DI float sum_rows(float v) {
  unsigned a = __builtin_bit_cast(unsigned, v), b = a;
  swap16(a, b);
  a = __builtin_bit_cast(unsigned, __builtin_bit_cast(float, a) + __builtin_bit_cast(float, b)); b = a;
  swap32(a, b);
  return __builtin_bit_cast(float, a) + __builtin_bit_cast(float, b);
}
constexpr int K1_WIN = 12;
constexpr unsigned OOB = 0x40000000u;
template <bool N0, bool N1>
DI void qkv_gemm(rsrc_t rs, unsigned vo, unsigned so, u32x4 (&a)[K1_WIN], const LAS char* b0, f32x4 (&acc)[2][4]) {
  constexpr int WIN = K1_WIN;
  const f32x4 z4 = {0.f, 0.f, 0.f, 0.f};
#pragma unroll
  for (int j = 0; j < 2; ++j)
#pragma unroll
    for (int t = 0; t < 4; ++t) acc[j][t] = z4;
  u32x4 bf[2][4];
#pragma unroll
  for (int t = 0; t < 4; ++t) bf[0][t] = lds128(b0 + t * 1024);
#pragma unroll 1
  for (int it = 0; it < 3; ++it) {
    const unsigned vn = it < 2 ? vo : OOB;
#pragma unroll
    for (int u = 0; u < 6; ++u) {
      const LAS char* bn = b0 + (u + 1) * 4096;
#pragma unroll
      for (int t = 0; t < 4; ++t) bf[(u + 1) & 1][t] = lds128(bn + t * 1024);
      SBAR();
#pragma unroll
      for (int j = 0; j < 2; ++j) {
#pragma unroll
        for (int t = 0; t < 4; ++t)
          acc[j][t] = (j == 0 ? N0 : N1) ? mfma16(bf[u & 1][t], a[2 * u + j], acc[j][t]) : mfma16(a[2 * u + j], bf[u & 1][t], acc[j][t]);
        SBAR();
        a[2 * u + j] = bld16(rs, vn, so + (j * 18 + 6 + u) * 1024);
        SBAR();
      }
    }
    b0 += 6 * 4096; so += 6 * 1024;
  }
}
template <bool GEMM, bool N0, bool N1>
DI void qkv_body(rsrc_t rs, unsigned vo, unsigned so, const u32x4 (&xv)[9], LAS char* hb, const LAS float* modv, int w, int lane,
                 f32x4 (&acc)[2][4] PROFARG) {
  u32x4 a[K1_WIN];
  const LAS float* gp = modv + (lane >> 4) * 8;
  f32x4 gs[3][4];
#pragma unroll
  for (int k = 0; k < 3; ++k) {
    const LAS float* p = gp + (k < 2 ? 2 * w + k : 16 + (w >> 2)) * 32;
    gs[k][0] = *(const LAS f32x4*)p; gs[k][1] = *(const LAS f32x4*)(p + 4);
    gs[k][2] = *(const LAS f32x4*)(p + 576); gs[k][3] = *(const LAS f32x4*)(p + 580);
  }
  SBAR();
  sfor<0, 9>(LAM(i_) {
    constexpr int i = i_.v, l0 = i < 3 ? 2 * i : i + 3, l1 = i < 3 ? 2 * i + 2 : i + 4;
    if constexpr (GEMM) sfor<l0, l1>(LAM(l_) { constexpr int l = l_.v; a[l] = bld16(rs, vo, so + ((l & 1) * 18 + (l >> 1)) * 1024); });
    SBAR();
    const u32x4 m = modulate8(xv[i], gs[i >> 2][0], gs[i >> 2][1], gs[i >> 2][2], gs[i >> 2][3]);
    *(LAS u32x4*)(hb + (i < 8 ? 8 * w + i : 64 + w) * 1024 + lane * 16) = m;
    SBAR();
  });
  TS(1, 2);
  LDSBAR();
  TS(1, 3);
  if constexpr (GEMM) qkv_gemm<N0, N1>(rs, vo, so, a, hb + lane * 16, acc);
}
__global__ void __launch_bounds__(512, 2) k1_qkv(char* __restrict__ ws) {
  __shared__ __attribute__((aligned(16))) char lds[73728 + 4608];
  const int tid = threadIdx.x, lane = tid & 63, w = __builtin_amdgcn_readfirstlane(tid >> 6);
  const int xcd = blockIdx.x & 7, idx = blockIdx.x >> 3;
  const int rb = (xcd >> 1) * 8 + (idx & 7), hq = (xcd & 1) * 4 + (idx >> 3);
  LAS char* hb = (LAS char*)lds;
  LAS float* modv = (LAS float*)(hb + 73728);
  const rsrc_t rs = ws_rsrc(ws);
  const unsigned vo = lane * 16;
  TSDECL; TS(1, 0);
  const bool domod = tid < 288;
  const unsigned mo = domod ? (unsigned)MODP_OFF + (tid < 144 ? D + 4 * tid : 4 * (tid - 144)) * 4 : OOB;
  u32x4 mp[4];
#pragma unroll
  for (int i = 0; i < 4; ++i) mp[i] = bld16(rs, mo, i * 3456 * 4);
  TS(1, 6);
  SBAR(); RAWBAR(); SBAR();
  TS(1, 7);
  u32x4 xv[9];
  {
    const unsigned no = (unsigned)NF_OFF + rb * 73728;
#pragma unroll
    for (int i = 0; i < 8; ++i) xv[i] = bld16(rs, vo, no + (8 * w + i) * 1024);
    xv[8] = bld16(rs, vo, no + (64 + w) * 1024);
  }
  SBAR();
  TS(1, 8);
  {
    const f32x4 ms = __builtin_bit_cast(f32x4, mp[0]) + __builtin_bit_cast(f32x4, mp[1]) + __builtin_bit_cast(f32x4, mp[2]) + __builtin_bit_cast(f32x4, mp[3]);
    const f32x4 mv = {rbf(ms[0]), rbf(ms[1]), rbf(ms[2]), rbf(ms[3])};
    *(LAS f32x4*)(domod ? (LAS char*)(modv + 4 * tid) : hb + 16 * tid) = tid < 144 ? gain4(mv) : mv;
  }
  LDSBAR();
  TS(1, 1);
  const unsigned so = (unsigned)WQKVT_OFF + (hq * 14 + 2 * w) * 18 * 1024;
  const int g = lane >> 4, c = lane & 15;
  f32x4 acc[2][4];
  if (w == 7) {
    qkv_body<false, false, false>(rs, vo, so, xv, hb, modv, w, lane, acc PROFPASS);
    const int i = blockIdx.x * 64 + lane;
    if (i < 864) {
      const f32x4 v = mod4((const float*)(ws + MODP_OFF), 4 * i);
      const int vec = i / 144;
      *(f32x4*)(ws + MODF_OFF + 16 * i) = (vec == 1 || vec == 4) ? gain4(v) : v;
    }
    if (blockIdx.x >= 64 && blockIdx.x < 128 && lane == 0) *(unsigned*)(ws + CNT_OFF + (blockIdx.x - 64) * 128) = 0u;
    TS(1, 4); TSEND(1);
    return;
  }
  if (w < 4) qkv_body<true, false, false>(rs, vo, so, xv, hb, modv, w, lane, acc PROFPASS);
  else if (w < 6) qkv_body<true, true, true>(rs, vo, so, xv, hb, modv, w, lane, acc PROFPASS);
  else qkv_body<true, false, true>(rs, vo, so, xv, hb, modv, w, lane, acc PROFPASS);
  TS(1, 4);
  const float sc = w < 2 || (w == 6 && g < 2) ? QSCALE : 1.f;
  unsigned o[2][4][2];
#pragma unroll
  for (int j = 0; j < 2; ++j) {
    const float f = (w == 6 && j == 1) ? 1.f : sc;
#pragma unroll
    for (int t = 0; t < 4; ++t) {
      o[j][t][0] = cvtpk(acc[j][t][0] * f, acc[j][t][1] * f);
      o[j][t][1] = cvtpk(acc[j][t][2] * f, acc[j][t][3] * f);
    }
  }
  const unsigned kvo = (unsigned)KV_OFF + rb * 2 * KVT;
  if (w < 4) {
#pragma unroll
    for (int j = 0; j < 2; ++j) {
      const int h = 4 * hq + 2 * (w & 1) + j;
      const unsigned base = w < 2 ? (unsigned)QLO_OFF + (h * 64 + rb * 2) * 1024 : kvo + h * KVH, st = w < 2 ? 1024 : KVT;
#pragma unroll
      for (int p = 0; p < 2; ++p) {
        swap16(o[j][2 * p][0], o[j][2 * p + 1][0]); swap16(o[j][2 * p][1], o[j][2 * p + 1][1]);
        bst16<AUX_K1>(rs, vo, base + p * st, u32x4{o[j][2 * p][0], o[j][2 * p][1], o[j][2 * p + 1][0], o[j][2 * p + 1][1]});
      }
    }
  } else {
#pragma unroll
    for (int j = 0; j < 2; ++j) {
      unsigned vv;
      unsigned base = kvo + 1152;
      if (w < 6) { base += (4 * hq + 2 * (w & 1) + j) * KVH; vv = c * 16; }
      else if (j == 0) continue;
      else {
        const int h = c < 8 ? c >> 1 : c & 3, d = c < 8 ? 16 + (c & 1) : c < 12 ? 18 : 19;
        vv = (4 * hq + h) * KVH + d * 16;
        if (c >= 8) {
          const unsigned cv = c < 12 ? 0x3F803F80u : 0u;
#pragma unroll
          for (int t = 0; t < 4; ++t) { o[1][t][0] = cv; o[1][t][1] = cv; }
        }
      }
      vv += (g >> 1) * 640 + (g & 1) * 320;
#pragma unroll
      for (int p = 0; p < 2; ++p) {
        swap32(o[j][2 * p][0], o[j][2 * p + 1][0]); swap32(o[j][2 * p][1], o[j][2 * p + 1][1]);
        bst16<AUX_K1>(rs, vv, base + p * KVT, u32x4{o[j][2 * p][0], o[j][2 * p][1], o[j][2 * p + 1][0], o[j][2 * p + 1][1]});
      }
    }
    if (w == 6) {
#pragma unroll
      for (int e = 0; e < 2; ++e) {
        const int h = 4 * hq + 2 * (g & 1) + e;
        const unsigned v0 = g < 2 ? (unsigned)QHI_OFF + (h * 2048 + rb * 64 + c) * 4 : kvo + h * KVH + 1024 + c * 4;
        const unsigned v1 = v0 + (g < 2 ? 128 : KVT);
#pragma unroll
        for (int t = 0; t < 4; ++t) bst4<AUX_K1>(rs, (t < 2 ? v0 : v1) + (t & 1) * 64, 0, o[0][t][e]);
      }
    }
  }
  TS(1, 5); TSEND(1);
}

#define ATT_ORDER 0
#define ATT_VAR 0
DI float ex2(float x) {
  return __builtin_amdgcn_exp2f(x);
}
typedef short s16x4v __attribute__((ext_vector_type(4)));
DI f32x16 mfma32k8(u32x4 a, u32x4 b, f32x16 c) {
  return mfma32(a, b, c);
}
DI f32x16 mfmapv(u32x4 a, u32x4 b, f32x16 c) {
  return mfma32(a, b, c);
}
DI u32x4 ldsv(const LAS char* p, u32x4 old, bool act) {
  return *(const LAS u32x4*)p;
}
#define ATT_G(P, i0, S, j0) \
  e0 = ex2(S[j0]); e1 = ex2(S[j0 + 1]); e2 = ex2(S[j0 + 2]); e3 = ex2(S[j0 + 3]); \
  SBAR(); P[i0] = cvtpk(e0, e1); P[i0 + 1] = cvtpk(e2, e3); SBAR();
DI void attn_step(const LAS char* kp, const LAS char* hp, const LAS char* vp, int off, u32x4& ka, u32x4& kb, u32x4& v0, u32x4& v1,
                  const u32x4 q0, const u32x4 q1, const f32x16& cin, f32x16& sc, f32x16& sn, u32x4& p1, f32x16& o, bool vact) {
  u32x4 p0;
  float e0, e1, e2, e3;
  ATT_G(p0, 0, sc, 0)
  o = mfmapv(v1, p1, o);
  SBAR();
  v1 = ldsv(vp + off + 640, v1, vact);
  ATT_G(p0, 2, sc, 4)
  sn = mfma32k8(kb, q1, sn);
  SBAR();
  kb[0] = *(const LAS unsigned*)(hp + off + 2 * KVT);
  ATT_G(p1, 0, sc, 8)
  o = mfmapv(v0, p0, o);
  SBAR();
  v0 = ldsv(vp + off + KVT, v0, vact);
  ATT_G(p1, 2, sc, 12)
  sc = mfma32(ka, q0, cin);
  SBAR();
  ka = lds128(kp + off + 3 * KVT);
}
template <bool SUBMAX, bool STAGED>
DI void attn_all(const LAS char* kv, u32x4 q0, u32x4 q1, float negm, int lane, f32x16& o) {
  const LAS char* kp = kv + lane * 16;
  const LAS char* hp = kv + 1024 + (lane & 31) * 4;
  const LAS char* vp = kv + 1152 + (lane >> 5) * 320 + (lane & 31) * 16;
  f32x16 cin;
#pragma unroll
  for (int i = 0; i < 16; ++i) cin[i] = SUBMAX ? negm : 0.f;
  u32x4 ka = lds128(kp);
  u32x4 kb = {*(const LAS unsigned*)(hp), 0u, 0u, 0u};
  f32x16 sa = mfma32(ka, q0, cin), sb;
  sa = mfma32k8(kb, q1, sa);
  ka = lds128(kp + KVT);
  sb = mfma32(ka, q0, cin);
  ka = lds128(kp + 2 * KVT);
  kb[0] = *(const LAS unsigned*)(hp + KVT);
  const bool vact = (lane & 31) < 20;
  const u32x4 zz = {0u, 0u, 0u, 0u};
  u32x4 v0 = ldsv(vp, zz, vact), v1 = zz, p1 = zz;
#pragma unroll 1
  for (int c = 0; c < 8; ++c) {
    if (STAGED) {
      if (c == 1 || c == 2) asm volatile("s_barrier" ::: "memory");
    }
#pragma unroll
    for (int u = 0; u < 8; u += 2) {
      attn_step(kp, hp, vp, u * KVT, ka, kb, v0, v1, q0, q1, cin, sa, sb, p1, o, vact);
      attn_step(kp, hp, vp, (u + 1) * KVT, ka, kb, v0, v1, q0, q1, cin, sb, sa, p1, o, vact);
    }
    kp += 8 * KVT; hp += 8 * KVT; vp += 8 * KVT;
  }
  o = mfmapv(v1, p1, o);
}
#define HELP_PRIO 0
#define HELP_MODE 2
template <int NU, bool ISW1>
DI void retile_units(const u16* __restrict__ w1, const u16* __restrict__ w2, const u16* __restrict__ wo, char* __restrict__ ws,
                     LAS char* stage, int job, int lane) {
  if (!ISW1) __builtin_amdgcn_s_setprio(2);
  u32x4 raw[NU][4];
  const int rr = lane >> 3, cc = (lane & 7) * 8;
#pragma unroll
  for (int u = 0; u < NU; ++u) {
    if (ISW1) {
      const int cgp = job / 6, s = (job % 6) * 3 + u;
#pragma unroll
      for (int i = 0; i < 4; ++i) raw[u][i] = *(const u32x4*)(w1 + (size_t)(32 * s + 8 * i + rr) * F + cgp * 64 + cc);
    } else {
      const int un = 3 * job + u;
      const bool isw2 = un < 1296;
      const int sg = un % 144, v = un - 1296, s = v % 18;
      const int n0 = isw2 ? (un / 144) * 64 : (v / 18) * 64;
#pragma unroll
      for (int i = 0; i < 4; ++i) {
        const int r = (8 * i + rr) & 15, g = r >> 2, j = 4 * (i >> 1) + (r & 3), ci = 4 * s + g, G = ci / 9, r9 = ci % 9;
        const int ko = r9 < 8 ? (4 * G + (r9 >> 1)) * 18 + 8 * (r9 & 1) + j : (4 * G + (j >> 1)) * 18 + 16 + (j & 1);
        const int k = isw2 ? 32 * sg + 8 * i + rr : ko;
        raw[u][i] = *(const u32x4*)((isw2 ? w2 : wo) + (size_t)k * D + n0 + cc);
      }
    }
  }
  SBAR();
  const int g = lane >> 4, c = lane & 15;
  u32x4 bv[ISW1 ? NU : 1];
  if (ISW1) {
#pragma unroll
    for (int u = 0; u < NU; ++u) {
      const float* mp = (const float*)(ws + MODF_OFF) + ((c & 1) ? 3 : 4) * D + 32 * ((job % 6) * 3 + u) + 8 * g;
      const f32x4 lo = *(const f32x4*)mp, hi = *(const f32x4*)(mp + 4);
      bv[u] = c < 2 ? u32x4{cvtpk(lo[0], lo[1]), cvtpk(lo[2], lo[3]), cvtpk(hi[0], hi[1]), cvtpk(hi[2], hi[3])} : u32x4{0u, 0u, 0u, 0u};
    }
  }
  const rsrc_t rs = ws_rsrc(ws);
  f32x4 acc[4];
#pragma unroll
  for (int i = 0; i < 4; ++i) acc[i] = f32x4{0.f, 0.f, 0.f, 0.f};
  const LAS char* rp = stage + (4 * g + (c >> 2)) * 128 + (lane & 3) * 8;
#pragma unroll
  for (int u = 0; u < NU; ++u) {
    u32x2 lo[4], hi[4];
    LAS char* sp = stage + (rr & 3) * 128 + (lane & 7) * 16;
    if (ISW1) {
      if (rr < 4) {
#pragma unroll
        for (int i = 0; i < 4; ++i) *(LAS u32x4*)(sp + i * 512) = raw[u][i];
      }
    } else {
      *(LAS u32x4*)(stage + lane * 16) = raw[u][0];
      *(LAS u32x4*)(stage + 1024 + lane * 16) = raw[u][1];
    }
#pragma unroll
    for (int i = 0; i < 4; ++i) lo[i] = tr_read(rp + i * 32);
    asm volatile("s_waitcnt lgkmcnt(0)" ::: "memory");
    if (ISW1) {
      if (rr >= 4) {
#pragma unroll
        for (int i = 0; i < 4; ++i) *(LAS u32x4*)(sp + i * 512) = raw[u][i];
      }
    } else {
      *(LAS u32x4*)(stage + lane * 16) = raw[u][2];
      *(LAS u32x4*)(stage + 1024 + lane * 16) = raw[u][3];
    }
#pragma unroll
    for (int i = 0; i < 4; ++i) hi[i] = tr_read(rp + i * 32);
    asm volatile("s_waitcnt lgkmcnt(0)" ::: "memory");
#pragma unroll
    for (int i = 0; i < 4; ++i) {
      const u32x4 fragv = {lo[i][0], lo[i][1], hi[i][0], hi[i][1]};
      size_t off;
      if (ISW1) {
        const int Tn = (job / 6) * 4 + i, s = (job % 6) * 3 + u;
        off = W1T_OFF + ((size_t)(Tn / 36) * 648 + frag_index1(Tn % 36, s)) * 1024;
        acc[i] = mfma16(fragv, bv[u], acc[i]);
      } else {
        const int un = 3 * job + u;
        if (un < 1296) {
          const int sg = un % 144;
          off = W2T_OFF + ((size_t)(sg / 18) * 648 + frag_index((un / 144) * 4 + i, sg % 18)) * 1024;
        } else {
          const int v = un - 1296;
          off = WOT_OFF + ((size_t)((v / 18) * 4 + i) * 18 + v % 18) * 1024;
        }
      }
      bst16<AUX_RT>(rs, lane * 16, (unsigned)off, fragv);
    }
    if (u == 0 && !ISW1) __builtin_amdgcn_s_setprio(0);
  }
  if (ISW1 && c < 2) {
#pragma unroll
    for (int i = 0; i < 4; ++i)
      *(f32x4*)(ws + CSB_OFF + ((size_t)((job % 6) * 2 + c) * F + ((job / 6) * 4 + i) * 16 + 4 * g) * 4) = acc[i];
  }
}
__global__ void __launch_bounds__(768, 3) k2_attn(const u16* __restrict__ w1, const u16* __restrict__ w2, const u16* __restrict__ wo,
                                                  char* __restrict__ ws) {
  __shared__ __attribute__((aligned(16))) char lds[163840];
  const int tid = threadIdx.x, lane = tid & 63, w = __builtin_amdgcn_readfirstlane(tid >> 6);
  const int xcd = blockIdx.x & 7, idx = blockIdx.x >> 3, h = 4 * xcd + (idx & 3), qb = idx >> 2;
  TSDECL; TS(2, 0);
  if (w >= 8) {
    __builtin_amdgcn_s_setprio(HELP_PRIO);
    {
      const char* src = ws + KV_OFF + (size_t)h * KVH + (w - 8) * 2048 + lane * 16;
      const unsigned lb = (unsigned)(size_t)(LAS char*)lds + (w - 8) * 2048;
      asm volatile("s_barrier" ::: "memory");
#pragma unroll
      for (int r = 4; r < 12; ++r) { glds16(src + r * 8192, lb + r * 8192); glds16(src + r * 8192 + 1024, lb + r * 8192 + 1024); }
      asm volatile("s_waitcnt vmcnt(12)\n\ts_barrier" ::: "memory");
#pragma unroll
      for (int r = 12; r < 19; ++r) { glds16(src + r * 8192, lb + r * 8192); glds16(src + r * 8192 + 1024, lb + r * 8192 + 1024); }
      asm volatile("s_waitcnt vmcnt(0)\n\ts_barrier" ::: "memory");
    }
    TS(2, 1);
    LAS char* stage = (LAS char*)lds + KVH + (w - 8) * 2048;
    const int job = blockIdx.x * 2 + (w & 1);
    if (w < 10) { if (job < 432) retile_units<3, true>(w1, w2, wo, ws, stage, job, lane); }
    else if (job < 486) retile_units<3, false>(w1, w2, wo, ws, stage, job, lane);
    TS(2, 2); TSEND(2);
    return;
  }
  const int qt = qb * 8 + w;
  u32x4 q0 = *(const u32x4*)(ws + QLO_OFF + ((size_t)h * 64 + qt) * 1024 + lane * 16);
  unsigned qh = *(const unsigned*)(ws + QHI_OFF + ((size_t)h * 2048 + qt * 32 + (lane & 31)) * 4);
  const char* src = ws + KV_OFF + (size_t)h * KVH + w * 1024 + lane * 16;
  const unsigned lb = (unsigned)(size_t)(LAS char*)lds + w * 1024;
#pragma unroll
  for (int r = 0; r < 4; ++r) glds16(src + r * 8192, lb + r * 8192);
  asm volatile("" : "+v"(q0), "+v"(qh));
  WAIT_VM(0);
  const u32x4 q1 = {lane < 32 ? qh : 0u, 0u, 0u, 0u};
  const LAS char* kv = (const LAS char*)lds;
  f32x16 o;
#pragma unroll
  for (int i = 0; i < 16; ++i) o[i] = 0.f;
  asm volatile("s_barrier" ::: "memory");
  TS(2, 1);
  attn_all<false, true>(kv, q0, q1, 0.f, lane, o);
  TS(2, 5);
  float l = __shfl(o[10], lane & 31);
  bool bad = !(l >= 7.8886091e-31f && l <= 1.3292280e36f);
#pragma unroll
  for (int i = 0; i < 10; ++i) bad |= !(__builtin_fabsf(o[i]) <= 3.0e38f) && (i < 8 || lane < 32);
  if (__builtin_expect(__any(bad), 0) && ATT_VAR < 3) {
    float m = -3.0e38f;
    const LAS char* kp = kv + lane * 16;
    const LAS char* hp = kv + 1024 + (lane & 31) * 4;
    f32x16 z;
#pragma unroll
    for (int i = 0; i < 16; ++i) z[i] = 0.f;
#pragma unroll 1
    for (int t = 0; t < 64; ++t) {
      const u32x4 ka = lds128(kp + t * KVT);
      const u32x4 kb = {*(const LAS unsigned*)(hp + t * KVT), 0u, 0u, 0u};
      f32x16 sc = mfma32(ka, q0, z);
      sc = mfma32(kb, q1, sc);
#pragma unroll
      for (int i = 0; i < 16; ++i) m = __builtin_fmaxf(m, sc[i]);
    }
    m = __builtin_fmaxf(m, __shfl_xor(m, 32));
#pragma unroll
    for (int i = 0; i < 16; ++i) o[i] = 0.f;
    attn_all<true, false>(kv, q0, q1, -m, lane, o);
    l = __shfl(o[10], lane & 31);
  }
  const float rl = 1.f / l;
  const int hf = lane >> 5, ci = 9 * (h >> 2) + 2 * (h & 3) + hf, ch = 9 * (h >> 2) + 8;
  const unsigned tokoff = ((lane >> 4) & 1) * 1024 + (lane & 15) * 16, ob = (unsigned)OF_OFF + qt * 36864;
  unsigned a0 = cvtpk(o[0] * rl, o[1] * rl), a1 = cvtpk(o[2] * rl, o[3] * rl), b0 = cvtpk(o[4] * rl, o[5] * rl), b1 = cvtpk(o[6] * rl, o[7] * rl);
  swap32(a0, b0); swap32(a1, b1);
  const rsrc_t rs = ws_rsrc(ws);
  bst16<AUX_O>(rs, (ci >> 2) * 2048 + (ci & 3) * 256 + tokoff, ob, u32x4{a0, a1, b0, b1});
  if (lane < 32) bst4<0>(rs, (ch >> 2) * 2048 + (ch & 3) * 256 + tokoff + (h & 3) * 4, ob, cvtpk(o[8] * rl, o[9] * rl));
  TS(2, 6); TSEND(2);
}

#define K4_X 0
#define K4_WIN 8
struct Silu { float z[4], e[4]; unsigned p[2]; };
constexpr int SOPS = 27;
#define PIN(x) asm volatile("" : "+v"(x))
template <int k>
DI void silu_op(Silu& q, const f32x4& v, const f32x4& cs, const f32x4& bs, float rs, float nm, LAS char* dst) {
  if constexpr (k < 4) { q.z[k] = __builtin_fmaf(cs[k], nm, bs[k]); PIN(q.z[k]); }
  else if constexpr (k < 8) { q.z[k - 4] = __builtin_fmaf(v[k - 4], rs, q.z[k - 4]); PIN(q.z[k - 4]); }
  else if constexpr (k < 12) { q.e[k - 8] = __builtin_amdgcn_exp2f(q.z[k - 8]); PIN(q.e[k - 8]); }
  else if constexpr (k < 16) { q.e[k - 12] = 1.f + q.e[k - 12]; PIN(q.e[k - 12]); }
  else if constexpr (k < 20) { q.e[k - 16] = __builtin_amdgcn_rcpf(q.e[k - 16]); PIN(q.e[k - 16]); }
  else if constexpr (k < 24) { q.e[k - 20] = q.z[k - 20] * q.e[k - 20]; PIN(q.e[k - 20]); }
  else if constexpr (k < 26) { q.p[k - 24] = cvtpk(q.e[2 * (k - 24)], q.e[2 * (k - 24) + 1]); PIN(q.p[k - 24]); }
  else *(LAS u32x2*)dst = u32x2{q.p[0], q.p[1]};
}
DI u32x4 bld16s(rsrc_t r, unsigned voff, unsigned soff) { return __builtin_amdgcn_raw_buffer_load_b128(r, voff, soff, 16); }
DI void wait_count(GAS unsigned* cnt) {
  for (unsigned spins = 0; spins < (1u << 20); ++spins) {
    if (__hip_atomic_load(cnt, __ATOMIC_RELAXED, __HIP_MEMORY_SCOPE_AGENT) >= 8u) break;
    __builtin_amdgcn_s_sleep(1);
  }
}
#define K4_XS 4
#define K4_W0 14
#define K4_SH0 16
constexpr int wdepth(int i) { return i < K4_SH0 ? K4_W0 : (K4_W0 - (i - K4_SH0) / 2 > K4_WIN ? K4_W0 - (i - K4_SH0) / 2 : K4_WIN); }
template <int NT>
DI void mlp_body(char* __restrict__ ws, u16* __restrict__ out, LAS char* hb, LAS char* h1, LAS float* st, LAS float* csb, LAS char* xk, int w, int lane, int fr, int rb PROFARG) {
  constexpr int NB = NT - 2, NF = NT * KS, WIN = K4_WIN;
  const int T0 = NT == 5 ? 5 * w : 20 + 4 * (w - 4);
  const int TA = 2 * w, TB = NT == 5 ? 16 + 3 * w : 28 + 2 * (w - 4);
  const unsigned wbase = (unsigned)(fr * 648 + (NT == 5 ? w * 90 : 360 + (w - 4) * 72)) * 1024u;
  const rsrc_t rs_ = ws_rsrc(ws);
  const unsigned vo = lane * 16;
  const unsigned o1 = (unsigned)W1T_OFF + wbase, o2 = (unsigned)W2T_OFF + wbase;
#define WLOAD(i) bld16(rs_, vo, (i) < NF ? o1 + (i) * 1024u : o2 + ((i) - NF) * 1024u)
  u32x4 fr_[2 * NF];
#define FRONT(i) ((i) + 1 + wdepth(i) < 2 * NF ? (i) + 1 + wdepth(i) : 2 * NF)
#define REFILL(i) sfor<FRONT((i) - 1), FRONT(i)>(LAM(k_) { fr_[k_.v] = WLOAD(k_.v); })
  sfor<0, FRONT(-1)>(LAM(k_) { fr_[k_.v] = WLOAD(k_.v); });
  SBAR();
  constexpr float NL2E = -1.4426950408889634f;
  if (NT == 5 && w == 0) wait_count((GAS unsigned*)(ws + CNT_OFF + rb * 128));
  asm volatile("s_barrier" ::: "memory");
  TS(4, 13);
  {
    u32x4 xg[9];
#pragma unroll
    for (int i = 0; i < 9; ++i) xg[i] = bld16s(rs_, vo, (unsigned)X1G_OFF + rb * 73728 + (w * 9 + i) * 1024);
    if (NT == 5 && w == 0) {
      f32x4 sv[4];
#pragma unroll
      for (int i = 0; i < 4; ++i)
        sv[i] = __builtin_bit_cast(f32x4, bld16s(rs_, lane * 32 + (i & 1) * 16, (unsigned)STAT_OFF + (rb * 2 + (i >> 1)) * 2048));
      const f32x4 t1 = sv[0] + sv[1], t2 = sv[2] + sv[3];
      const float s1 = (t1[0] + t1[1]) + (t1[2] + t1[3]), s2 = (t2[0] + t2[1]) + (t2[2] + t2[3]);
      const float mean = s1 * (1.f / D);
      const float var = __builtin_fmaxf(s2 * (1.f / D) - mean * mean, 0.f);
      const float rstd = __builtin_amdgcn_rsqf(var + 1e-5f);
      st[2 * lane] = rstd * NL2E; st[2 * lane + 1] = -mean * rstd;
    }
#pragma unroll
    for (int i = 0; i < 9; ++i) *(LAS u32x4*)(hb + (w * 9 + i) * 1024 + lane * 16) = xg[i];
  }
  LDSBAR();
  TS(4, 2);
  unsigned bp0 = (unsigned)(size_t)hb + lane * 16, bp1 = bp0 + 40960, hp0 = (unsigned)(size_t)h1 + lane * 16, hp1 = hp0 + 40960;
  asm volatile("" : "+v"(bp0), "+v"(bp1), "+v"(hp0), "+v"(hp1));
#define XFRAG(f) lds128((const LAS char*)(size_t)((f) < 40 ? bp0 + (f) * 1024 : bp1 + ((f) - 40) * 1024))
#define HFRAG(f) lds128((const LAS char*)(size_t)((f) < 40 ? hp0 + (f) * 1024 : hp1 + ((f) - 40) * 1024))
  const int g = lane >> 4, c = lane & 15;
  f32x4 accA[2][4], accB[NB][4], acc[NT][4];
  u32x4 bf[2][4];
  const f32x4 z4 = {0.f, 0.f, 0.f, 0.f};
#pragma unroll
  for (int t = 0; t < 4; ++t) bf[0][t] = XFRAG(t);
  sfor<0, KS>(LAM(s_) {
    constexpr int s = s_.v;
#pragma unroll
    for (int t = 0; t < 4; ++t) bf[(s + 1) & 1][t] = XFRAG(((s + 1) % KS) * 4 + t);
    SBAR();
    sfor<0, 2>(LAM(j_) {
      constexpr int j = j_.v, i = s * 2 + j;
#pragma unroll
      for (int t = 0; t < 4; ++t) { accA[j][t] = mfma16(fr_[i], bf[s & 1][t], s == 0 ? z4 : accA[j][t]); PIN(accA[j][t]); }
      SBAR();
      REFILL(i);
      SBAR();
    });
  });
  TS(4, 3);
  float rs[4], nm[4];
#pragma unroll
  for (int t = 0; t < 4; ++t) { rs[t] = st[2 * (t * 16 + c)]; nm[t] = st[2 * (t * 16 + c) + 1]; }
  const LAS float* csg = csb + 4 * g;
  f32x4 cs[2], bs[2];
  cs[0] = *(const LAS f32x4*)(csg + TA * 16); bs[0] = *(const LAS f32x4*)(csg + 576 + TA * 16);
  cs[1] = *(const LAS f32x4*)(csg + TA * 16 + 16); bs[1] = *(const LAS f32x4*)(csg + 576 + TA * 16 + 16);
  Silu sq;
  sfor<0, KS>(LAM(s_) {
    constexpr int s = s_.v, M = 16 * NB * 4, TOT = 8 * SOPS;
    if constexpr (s + 1 < KS) {
#pragma unroll
      for (int t = 0; t < 4; ++t) bf[(s + 1) & 1][t] = XFRAG((s + 1) * 4 + t);
    }
    SBAR();
    sfor<0, NB>(LAM(j_) {
      constexpr int j = j_.v, i = 36 + s * NB + j;
      sfor<0, 4>(LAM(t_) {
        constexpr int t = t_.v, m = (s * NB + j) * 4 + t;
        accB[j][t] = mfma16(fr_[i], bf[s & 1][t], s == 0 ? z4 : accB[j][t]);
        PIN(accB[j][t]);
        if constexpr (m < M) {
          sfor<m * TOT / M, (m + 1) * TOT / M>(LAM(o_) {
            constexpr int o = o_.v, q = o / SOPS, jj = q >> 2, tt = q & 3;
            silu_op<o % SOPS>(sq, accA[jj][tt], cs[jj], bs[jj], rs[tt], nm[tt], h1 + (w * 4 + tt) * 1024 + lane * 16 + jj * 8);
          });
        }
        SBAR();
      });
      REFILL(i);
      SBAR();
    });
  });
  cs[0] = *(const LAS f32x4*)(csg + TB * 16); bs[0] = *(const LAS f32x4*)(csg + 576 + TB * 16);
  cs[1] = *(const LAS f32x4*)(csg + TB * 16 + 16); bs[1] = *(const LAS f32x4*)(csg + 576 + TB * 16 + 16);
  asm volatile("s_waitcnt lgkmcnt(0)\n\ts_barrier" ::: "memory");
  TS(4, 5);
#pragma unroll
  for (int t = 0; t < 4; ++t) bf[0][t] = HFRAG(t);
  sfor<0, 8>(LAM(s_) {
    constexpr int s = s_.v, M = 8 * NT * 4, TOT = NB * 4 * SOPS;
    if constexpr (s + 1 < 8) {
#pragma unroll
      for (int t = 0; t < 4; ++t) bf[(s + 1) & 1][t] = HFRAG((s + 1) * 4 + t);
    }
    SBAR();
    sfor<0, NT>(LAM(j_) {
      constexpr int j = j_.v, i = NF + s * NT + j;
      sfor<0, 4>(LAM(t_) {
        constexpr int t = t_.v, m = (s * NT + j) * 4 + t;
        acc[j][t] = mfma16(fr_[i], bf[s & 1][t], s == 0 ? z4 : acc[j][t]);
        PIN(acc[j][t]);
        sfor<m * TOT / M, (m + 1) * TOT / M>(LAM(o_) {
          constexpr int o = o_.v, q = o / SOPS, jj = q >> 2, tt = q & 3;
          const int T = TB + jj;
          if constexpr (NB == 3 && o == 4 * SOPS + 8) {
            cs[0] = *(const LAS f32x4*)(csg + TB * 16 + 32); bs[0] = *(const LAS f32x4*)(csg + 576 + TB * 16 + 32);
          }
          silu_op<o % SOPS>(sq, accB[jj][tt], cs[jj & 1], bs[jj & 1], rs[tt], nm[tt], h1 + ((T >> 1) * 4 + tt) * 1024 + lane * 16 + (T & 1) * 8);
        });
        SBAR();
      });
      REFILL(i);
      SBAR();
    });
  });
  asm volatile("s_waitcnt lgkmcnt(0)\n\ts_barrier" ::: "memory");
  TS(4, 7);
#pragma unroll
  for (int t = 0; t < 4; ++t) bf[0][t] = HFRAG(32 + t);
  sfor<8, KS>(LAM(s_) {
    constexpr int s = s_.v;
    if constexpr (s + 1 < KS) {
#pragma unroll
      for (int t = 0; t < 4; ++t) bf[(s + 1) & 1][t] = HFRAG((s + 1) * 4 + t);
    }
    SBAR();
    sfor<0, NT>(LAM(j_) {
      constexpr int j = j_.v, i = NF + s * NT + j;
#pragma unroll
      for (int t = 0; t < 4; ++t) { acc[j][t] = mfma16(fr_[i], bf[s & 1][t], acc[j][t]); PIN(acc[j][t]); }
      SBAR();
      REFILL(i);
      SBAR();
    });
  });
  TS(4, 8);
#pragma unroll
  for (int j = 0; j < NT; ++j) {
    const int T = T0 + j;
#pragma unroll
    for (int tp = 0; tp < 2; ++tp) {
      const f32x4 v0 = acc[j][2 * tp], v1 = acc[j][2 * tp + 1];
      u32x4 o = {cvtpk(v0[0], v0[1]), cvtpk(v0[2], v0[3]), cvtpk(v1[0], v1[1]), cvtpk(v1[2], v1[3])};
      const int ul = T * 2 + tp - fr * 9;
      if (ul >= 0 && ul < 9) *(LAS u32x4*)(hb + ul * 1024 + vo) = o;
      else bst16<AUX_PART>(rs_, vo, (unsigned)(PART_OFF + ((((size_t)fr * 32 + rb) * 36 + T) * 2 + tp) * 1024), o);
    }
  }
  TS(4, 9);
  const int u0 = fr * 9 + w;
  f32x4 gb = *(const f32x4*)(ws + MODF_OFF + (5 * D + (u0 >> 1) * 16 + 4 * g) * 4), gb2;
  if (w == 0) gb2 = *(const f32x4*)(ws + MODF_OFF + (5 * D + ((u0 + 8) >> 1) * 16 + 4 * g) * 4);
  asm volatile("s_waitcnt vmcnt(0) lgkmcnt(0)\n\ts_barrier" ::: "memory");
  TS(4, 10);
  if (NT == 5 && w == 0) {
    GAS unsigned* cnt = (GAS unsigned*)(ws + CNT_OFF + (32 + rb) * 128);
    if (lane == 0) __hip_atomic_fetch_add(cnt, 1u, __ATOMIC_RELAXED, __HIP_MEMORY_SCOPE_AGENT);
    wait_count(cnt);
  }
  asm volatile("s_barrier" ::: "memory");
  TS(4, 11);
  auto request = [&](int ul, u32x4 (&p)[7]) __attribute__((always_inline)) {
#pragma unroll
    for (int i = 0; i < 7; ++i) p[i] = bld16s(rs_, vo, (unsigned)PART_OFF + ((((fr + 1 + i) & 7) * 32 + rb) * 72 + fr * 9 + ul) * 1024);
  };
  auto reduce = [&](int ul, const u32x4 (&p)[7], const f32x4& gv) __attribute__((always_inline)) {
    const u32x4 own = lds128(hb + ul * 1024 + vo), xv = lds128(xk + ul * 1024 + vo);
    const f32x4 gm = gv * -0.6931471805599453f;
    const int unit = fr * 9 + ul, col = (unit >> 1) * 16 + 4 * g;
#pragma unroll
    for (int t = 0; t < 2; ++t) {
      float a[4] = {bflo(own[2 * t]), bfhi(own[2 * t]), bflo(own[2 * t + 1]), bfhi(own[2 * t + 1])};
#pragma unroll
      for (int f = 0; f < 7; ++f) {
        a[0] += bflo(p[f][2 * t]); a[1] += bfhi(p[f][2 * t]);
        a[2] += bflo(p[f][2 * t + 1]); a[3] += bfhi(p[f][2 * t + 1]);
      }
      u32x2 o = {cvtpk(bflo(xv[2 * t]) + gm[0] * a[0], bfhi(xv[2 * t]) + gm[1] * a[1]),
                 cvtpk(bflo(xv[2 * t + 1]) + gm[2] * a[2], bfhi(xv[2 * t + 1]) + gm[3] * a[3])};
      *(u32x2*)(out + (size_t)(rb * 64 + (unit & 1) * 32 + t * 16 + c) * D + col) = o;
    }
  };
  u32x4 p[7];
  request(w, p);
  if (NT == 5 && w == 0) {
    u32x4 p2[7];
    request(8, p2);
    SBAR();
    reduce(0, p, gb);
    reduce(8, p2, gb2);
  } else {
    reduce(w, p, gb);
  }
  TS(4, 12); TSEND(4);
#undef WLOAD
#undef FRONT
#undef REFILL
#undef XFRAG
#undef HFRAG
}
#define K4_MAP 2
__global__ void __launch_bounds__(512, 2) k4_mlp(const u16* __restrict__ x, char* __restrict__ ws, u16* __restrict__ out) {
  __shared__ __attribute__((aligned(16))) char lds[2 * 73728 + 512 + 4608 + 9216];
  const int tid = threadIdx.x, lane = tid & 63, w = __builtin_amdgcn_readfirstlane(tid >> 6);
  const int xcd = blockIdx.x & 7, idx = blockIdx.x >> 3;
  const int rb = (xcd >> 1) * 8 + (idx & 7), fr = (xcd & 1) * 4 + (idx >> 3);
  LAS char* hb = (LAS char*)lds;
  LAS char* h1 = hb + 73728;
  LAS float* st = (LAS float*)(hb + 2 * 73728);
  LAS float* csb = st + 128;
  LAS char* xk = (LAS char*)(csb + 1152);
  LAS float* red = (LAS float*)hb;
  TSDECL; TS(4, 0);
  const rsrc_t rs = ws_rsrc(ws);
  const unsigned vo = lane * 16;
  const int g = lane >> 4, c = lane & 15;
  {
    const char* src = ws + OF_OFF + (size_t)rb * 73728 + w * 9216 + lane * 16;
    const unsigned lb = (unsigned)(size_t)h1 + w * 9216;
#pragma unroll
    for (int i = 0; i < 9; ++i) glds16(src + i * 1024, lb + i * 1024);
  }
  if (w < 5) {
    const int Tf = (9 * fr + 1) >> 1, T = w < 4 ? Tf + w : (fr & 1) ? Tf - 1 : Tf + 4;
    constexpr int W3 = 10;
    const unsigned ao = (unsigned)WOT_OFF + T * 18 * 1024;
    u32x4 a[18];
    SBAR();
#pragma unroll
    for (int s = 0; s < W3; ++s) a[s] = bld16(rs, vo, ao + s * 1024);
    SBAR();
    asm volatile("s_waitcnt vmcnt(%0)\n\ts_barrier" :: "i"(W3) : "memory");
    TS(4, 1);
    u32x2 xr[4];
    f32x4 ga, gb2;
    f32x4 acc[4];
#pragma unroll
    for (int t = 0; t < 4; ++t) acc[t] = f32x4{0.f, 0.f, 0.f, 0.f};
    const LAS char* bp = h1 + vo;
    u32x4 b[2][4];
#pragma unroll
    for (int t = 0; t < 4; ++t) b[0][t] = lds128(bp + (t >> 1) * 36864 + (t & 1) * 1024);
#pragma unroll
    for (int s = 0; s < 18; ++s) {
      if (s + W3 < 18) a[s + W3] = bld16(rs, vo, ao + (s + W3) * 1024);
      if (s >= K4_XS && s < K4_XS + 4) xr[s - K4_XS] = *(const u32x2*)(x + (size_t)(rb * 64 + (s - K4_XS) * 16 + c) * D + T * 16 + 4 * g);
      if (s == K4_XS + 4) ga = *(const f32x4*)(ws + MODF_OFF + (2 * D + T * 16 + 4 * g) * 4);
      if (s == K4_XS + 5) gb2 = *(const f32x4*)(ws + MODF_OFF + (4 * D + T * 16 + 4 * g) * 4);
      if (s + 1 < 18) {
#pragma unroll
        for (int t = 0; t < 4; ++t) b[(s + 1) & 1][t] = lds128(bp + (t >> 1) * 36864 + ((s + 1) * 2 + (t & 1)) * 1024);
      }
      SBAR();
#pragma unroll
      for (int t = 0; t < 4; ++t) acc[t] = mfma16(a[s], b[s & 1][t], acc[t]);
      SBAR();
    }
    TS(4, 4);
#pragma unroll
    for (int tp = 0; tp < 2; ++tp) {
      const bool own = w < 4 || tp == (fr & 1);
      u32x4 xo;
      unsigned og[2][2];
#pragma unroll
      for (int t = 0; t < 2; ++t) {
        const int tt = 2 * tp + t;
        const unsigned o0 = cvtpk(bflo(xr[tt][0]) + ga[0] * acc[tt][0], bfhi(xr[tt][0]) + ga[1] * acc[tt][1]);
        const unsigned o1 = cvtpk(bflo(xr[tt][1]) + ga[2] * acc[tt][2], bfhi(xr[tt][1]) + ga[3] * acc[tt][3]);
        xo[2 * t] = o0; xo[2 * t + 1] = o1;
        const float v0 = bflo(o0), v1 = bfhi(o0), v2 = bflo(o1), v3 = bfhi(o1);
        og[t][0] = cvtpk(v0 * gb2[0], v1 * gb2[1]); og[t][1] = cvtpk(v2 * gb2[2], v3 * gb2[3]);
        const float s1 = sum_rows((v0 + v1) + (v2 + v3)), s2 = sum_rows((v0 * v0 + v1 * v1) + (v2 * v2 + v3 * v3));
        if (g == 0) { red[(w * 2) * 64 + tt * 16 + c] = own ? s1 : 0.f; red[(w * 2 + 1) * 64 + tt * 16 + c] = own ? s2 : 0.f; }
      }
      swap16(og[0][0], og[1][0]); swap16(og[0][1], og[1][1]);
      if (own) {
        *(LAS u32x4*)(xk + (2 * T + tp - 9 * fr) * 1024 + vo) = xo;
        bst16<16>(rs, (g & 1) * 1024 + ((2 * (T & 1) + (g >> 1)) * 16 + c) * 16,
                  (unsigned)X1G_OFF + rb * 73728 + ((T >> 1) * 4 + tp * 2) * 1024, u32x4{og[0][0], og[0][1], og[1][0], og[1][1]});
      }
    }
  } else {
    asm volatile("s_waitcnt vmcnt(0)\n\ts_barrier" ::: "memory");
    const int i0 = (w - 5) * 64 + lane, i1 = i0 + 192;
    const unsigned v0 = ((i0 >= 144) * F + fr * 576 + 4 * (i0 >= 144 ? i0 - 144 : i0)) * 4;
    const unsigned v1 = i1 < 288 ? (F + fr * 576 + 4 * (i1 - 144)) * 4 : OOB;
    u32x4 c0[6], c1[6];
#pragma unroll
    for (int k = 0; k < 6; ++k) { c0[k] = bld16(rs, v0, (unsigned)CSB_OFF + k * 2 * F * 4); c1[k] = bld16(rs, v1, (unsigned)CSB_OFF + k * 2 * F * 4); }
    f32x4 s0 = __builtin_bit_cast(f32x4, c0[0]), s1 = __builtin_bit_cast(f32x4, c1[0]);
#pragma unroll
    for (int k = 1; k < 6; ++k) { s0 += __builtin_bit_cast(f32x4, c0[k]); s1 += __builtin_bit_cast(f32x4, c1[k]); }
    *(LAS f32x4*)(csb + 4 * i0) = s0 * -1.4426950408889634f;
    if (i1 < 288) *(LAS f32x4*)(csb + 4 * i1) = s1 * -1.4426950408889634f;
  }
  LDSBAR();
  if (tid < 128) {
    float sm = 0.f;
#pragma unroll
    for (int i = 0; i < 5; ++i) sm += red[(i * 2 + (tid >> 6)) * 64 + (tid & 63)];
    bst4<16>(rs, ((rb * 2 + (tid >> 6)) * 64 + (tid & 63)) * 32 + fr * 4, (unsigned)STAT_OFF, __builtin_bit_cast(unsigned, sm));
  }
  asm volatile("s_waitcnt vmcnt(0)\n\ts_barrier" ::: "memory");
  if (tid == 0) __hip_atomic_fetch_add((GAS unsigned*)(ws + CNT_OFF + rb * 128), 1u, __ATOMIC_RELAXED, __HIP_MEMORY_SCOPE_AGENT);
  TS(4, 6);
  if (w < 4) mlp_body<5>(ws, out, hb, h1, st, csb, xk, w, lane, fr, rb PROFPASS);
  else mlp_body<4>(ws, out, hb, h1, st, csb, xk, w, lane, fr, rb PROFPASS);
}

extern "C" void kernel_launch(void* const* d_in, const int* in_sizes, int n_in, void* d_out, int out_size, void* d_ws,
                              size_t ws_size, hipStream_t stream) {
  const u16* x = (const u16*)d_in[0];
  const u16* wqkv = (const u16*)d_in[1];
  const u16* wo = (const u16*)d_in[2];
  const u16* w1 = (const u16*)d_in[4];
  const u16* w2 = (const u16*)d_in[5];
  const u16* c = (const u16*)d_in[6];
  const u16* wmod = (const u16*)d_in[7];
  char* ws = (char*)d_ws;
  k0_prep<<<K0_MOD + K0_LN + K0_QKV_BLKS, 256, 0, stream>>>(c, wmod, wqkv, x, ws);
  k1_qkv<<<256, 512, 0, stream>>>(ws);
  k2_attn<<<256, 768, 0, stream>>>(w1, w2, wo, ws);
  k4_mlp<<<256, 512, 0, stream>>>(x, ws, (u16*)d_out);
}
```
